```python
import torch

def _softmax(x, axis=-1):
    return torch.softmax(x, dim=axis)

def _gelu(x):
    return torch.nn.functional.gelu(x, approximate='tanh')

def _silu(x):
    return torch.nn.functional.silu(x)

def _sigmoid(x):
    return torch.sigmoid(x)

def _layernorm(x, axis=-1, eps=1e-05):
    m = x.mean(dim=axis, keepdim=True)
    v = x.var(dim=axis, keepdim=True, unbiased=False)
    return (x - m) / torch.sqrt(v + eps)

def _scatter_add(tgt, idx, src, axis=0):
    return tgt.clone().index_add_(axis, idx, src)

def _segment_sum(src, seg, n):
    out = torch.zeros((n,) + src.shape[1:], dtype=src.dtype)
    return out.index_add_(0, seg, src)

def _pad(x, pw):
    flat = tuple((v for pair in reversed(pw) for v in pair))
    return torch.nn.functional.pad(x, flat)
kernel_name = 'standalone__molecular_angular_painn__graph'

def setup_inputs():

    _w = lambda a: (lambda t: t.to(torch.bfloat16) if t.is_floating_point() else t)(torch.as_tensor(a))
    N, deg, D = (25000, 16, 128)
    n_rbf = max(4, 4)
    E = N * deg
    cutoff = 5.0
    freqs = (torch.arange(1, n_rbf + 1) * torch.pi / cutoff).to(float)
    return {'sv': _w((lambda *s: torch.randn(*s, dtype=torch.bfloat16) * 0.4)(N, 4, D) * 0.3), 'pos': _w((lambda *s: torch.randn(*s, dtype=torch.bfloat16) * 3.2)(N, 3) * 1.2), 'src': _w((lambda lo, hi, *s: torch.randint(lo, hi, s))(0, N, E).to(torch.int64)), 'dst': _w((lambda lo, hi, *s: torch.randint(lo, hi, s))(0, N, E).to(torch.int64)), 'freqs': _w(freqs), 'w_phi1': _w((lambda *s: torch.randn(*s, dtype=torch.bfloat16) * 3.2)(D, D) / D ** 0.5), 'w_phi2': _w((lambda *s: torch.randn(*s, dtype=torch.bfloat16) * 3.2)(D, 3 * D) / D ** 0.5), 'w_rbf': _w((lambda *s: torch.randn(*s, dtype=torch.bfloat16) * 3.2)(n_rbf, 3 * D) / n_rbf ** 0.5)}

def reference(sv, pos, src, dst, freqs, w_phi1, w_phi2, w_rbf):
    N, _, D = sv.shape
    E = src.shape[0]
    PI, cutoff = (3.141592653589793, 5.0)
    s, v = (sv[:, 0, :].clone(), sv[:, 1:, :].clone())
    r_vec = pos[src] - pos[dst]
    d = torch.sqrt((r_vec * r_vec).sum(-1) + 1e-08)
    r_hat = r_vec / d[:, None]
    rbf = torch.sin(d[:, None] * freqs[None, :]) / d[:, None]
    fcut = 0.5 * (torch.cos(PI * d / cutoff) + 1.0)
    fcut = torch.where(d < cutoff, fcut, 0.0 * d)
    Wf = rbf @ w_rbf * fcut[:, None]
    phi = (_silu(s @ w_phi1) @ w_phi2)[src]
    m = phi * Wf
    m_s, m_vv, m_vr = (m[:, :D].clone(), m[:, D:2 * D].clone(), m[:, 2 * D:].clone())
    ds = _segment_sum(m_s, dst, N)
    dv_e = m_vv[:, None, :] * v[src] + m_vr[:, None, :] * r_hat[:, :, None]
    dv = _segment_sum(dv_e.reshape(E, 3 * D), dst, N).reshape(N, 3, D)
    return sv + torch.concatenate([ds[:, None, :], dv], axis=1)
```

```cpp
#include <hip/hip_runtime.h>
#include <stdint.h>

typedef unsigned short u16;
typedef unsigned int u32;
typedef unsigned long long u64;
typedef __attribute__((ext_vector_type(8))) __bf16 bf16x8;
typedef __attribute__((ext_vector_type(2))) __bf16 bf16x2;
typedef __attribute__((ext_vector_type(4))) float f32x4;
typedef __attribute__((ext_vector_type(4))) u32 u32x4;
typedef __attribute__((ext_vector_type(2))) u32 u32x2;
typedef __attribute__((ext_vector_type(4))) int i32x4;
typedef __attribute__((ext_vector_type(4))) short s16x4;
typedef __attribute__((ext_vector_type(2))) u16 u16x2;
typedef __attribute__((address_space(3))) char lds_char;

#define DI __device__ __forceinline__

constexpr int TILES = 256;
constexpr int G = 128;
constexpr int PRE_T = 1024;
constexpr int NODES_PB = 256;
constexpr int W2_BLOCKS = 6;
constexpr int CAP = 512;
constexpr int PITCH = 1056;
constexpr int CHUNK_B = 32 * PITCH;
constexpr int NBUF = 3;

constexpr int L_BUF = 0;
constexpr int L_WFA = NBUF * CHUNK_B;
constexpr int L_RH = L_WFA + CAP * 16;
constexpr int L_SRC = L_RH + 3 * CAP * 4;
constexpr int L_LIST = L_SRC + CAP * 4;
constexpr int L_DL = L_LIST + CAP * 4;
constexpr int L_TOT = L_DL + CAP * 2;
constexpr int MAIN_LDS = L_TOT + 64;

DI float bf2f(u32 b) { return __uint_as_float(b << 16); }
DI float rbf16(float x) { return (float)(__bf16)x; }
DI u32 pack2(float a, float b) { bf16x2 v; v[0] = (__bf16)a; v[1] = (__bf16)b; return __builtin_bit_cast(u32, v); }
DI u32 f2bf(float a) { return (u32)__builtin_bit_cast(u16, (__bf16)a); }
DI float lo16(u32 p) { return __uint_as_float(p << 16); }
DI float hi16(u32 p) { return __uint_as_float(p & 0xffff0000u); }

DI u32 wave_incl_scan(u32 v) {
    v += (u32)__builtin_amdgcn_update_dpp(0, (int)v, 0x111, 0xf, 0xf, false);
    v += (u32)__builtin_amdgcn_update_dpp(0, (int)v, 0x112, 0xf, 0xf, false);
    v += (u32)__builtin_amdgcn_update_dpp(0, (int)v, 0x114, 0xf, 0xf, false);
    v += (u32)__builtin_amdgcn_update_dpp(0, (int)v, 0x118, 0xf, 0xf, false);
    v += (u32)__builtin_amdgcn_update_dpp(0, (int)v, 0x142, 0xa, 0xf, false);
    v += (u32)__builtin_amdgcn_update_dpp(0, (int)v, 0x143, 0xc, 0xf, false);
    return v;
}

DI void dma16(const void* src, char* lds_wave_base) {
    __builtin_amdgcn_global_load_lds((const __attribute__((address_space(1))) void*)src,
                                     (__attribute__((address_space(3))) void*)lds_wave_base, 16, 0, 0);
}
DI void lds_barrier() {
    asm volatile("s_waitcnt lgkmcnt(0)" ::: "memory");
    __builtin_amdgcn_s_barrier();
    asm volatile("" ::: "memory");
}
typedef __attribute__((ext_vector_type(4))) short v4i16_t;
DI u32x2 tr_read(const char* p) {
    v4i16_t r = __builtin_amdgcn_ds_read_tr16_b64_v4i16((__attribute__((address_space(3))) v4i16_t*)p);
    return __builtin_bit_cast(u32x2, r);
}
DI f32x4 mfma16(u32x4 a, u32x4 b, f32x4 c) {
    return __builtin_amdgcn_mfma_f32_16x16x32_bf16(__builtin_bit_cast(bf16x8, a), __builtin_bit_cast(bf16x8, b), c, 0, 0, 0);
}

__global__ __launch_bounds__(PRE_T) void pre_kernel(const u16* __restrict__ sv, const u16* __restrict__ pos,
                                                    const int* __restrict__ src, const int* __restrict__ dst,
                                                    const u16* __restrict__ w1, const u16* __restrict__ w2,
                                                    u16* __restrict__ h, u32x4* __restrict__ w2f, u32* __restrict__ offs,
                                                    u32x4* __restrict__ recs, int N, int E, int EPB, int NT, u32 ntmagic,
                                                    int nNodeBlocks) {
    extern __shared__ __attribute__((aligned(16))) char smem[];
    const int t = threadIdx.x, lane = t & 63;
    const int w = __builtin_amdgcn_readfirstlane(t >> 6);
    const int b = blockIdx.x;
    if (b < G) {
        const int g = (b & 7) * (G / 8) + (b >> 3);
        const int posUnits = (N * 6 + 15) >> 4;
        const int posBytes = posUnits * 16;
        u32* hist = (u32*)(smem + posBytes);
        u32* wsum = hist + 2048;
        for (int u0 = w * 64; u0 < posUnits; u0 += PRE_T) {
            const int u = u0 + lane;
            if (u < posUnits) dma16((const char*)pos + (size_t)u * 16, smem + (size_t)u0 * 16);
        }
        const int e0 = g * EPB + t * 4;
        const int eEnd = min(E, (g + 1) * EPB);
        int s4[4], d4[4];
        if (e0 + 3 < eEnd) {
            const i32x4 a = *(const i32x4*)(src + e0), c = *(const i32x4*)(dst + e0);
#pragma unroll
            for (int i = 0; i < 4; ++i) { s4[i] = a[i]; d4[i] = c[i]; }
        } else {
#pragma unroll
            for (int i = 0; i < 4; ++i) { const bool ok = e0 + i < eEnd; s4[i] = ok ? src[e0 + i] : -1; d4[i] = ok ? dst[e0 + i] : 0; }
        }
        hist[2 * t] = 0; hist[2 * t + 1] = 0;
        __syncthreads();
        const u32* posw = (const u32*)smem;
        u32 key[4], rank[4], rA[4], rB[4], rC[4];
        bool act[4];
#pragma unroll
        for (int i = 0; i < 4; ++i) {
            act[i] = false; key[i] = 0; rank[i] = 0; rA[i] = 0; rB[i] = 0; rC[i] = 0;
            if (s4[i] >= 0) {
                const int si = s4[i], di = d4[i];
                const u32 as0 = posw[(3 * si) >> 1], as1 = posw[((3 * si) >> 1) + 1];
                const u32 ad0 = posw[(3 * di) >> 1], ad1 = posw[((3 * di) >> 1) + 1];
                const u64 ps = (((u64)as1 << 32) | as0) >> ((si & 1) * 16);
                const u64 pd = (((u64)ad1 << 32) | ad0) >> ((di & 1) * 16);
                const float rx = rbf16(bf2f((u32)ps & 0xffff) - bf2f((u32)pd & 0xffff));
                const float ry = rbf16(bf2f((u32)(ps >> 16) & 0xffff) - bf2f((u32)(pd >> 16) & 0xffff));
                const float rz = rbf16(bf2f((u32)(ps >> 32) & 0xffff) - bf2f((u32)(pd >> 32) & 0xffff));
                const float ss = rbf16(rbf16(rx * rx) + rbf16(ry * ry) + rbf16(rz * rz));
                const float d = rbf16(sqrtf(rbf16(ss + 1e-8f)));
                const bool a = !(d >= 5.0f && d <= 3.4e38f);
                if (a) {
                    const u32 tile = __umulhi((u32)di, ntmagic);
                    const u32 loc = (u32)di - tile * (u32)NT;
                    key[i] = tile * 8 + (loc >> 4);
                    rank[i] = atomicAdd(&hist[key[i]], 1u);
                    rA[i] = loc | (__float_as_uint(d) & 0xffff0000u);
                    rB[i] = (__float_as_uint(rx) >> 16) | (__float_as_uint(ry) & 0xffff0000u);
                    rC[i] = __float_as_uint(rz) >> 16;
                    act[i] = true;
                }
            }
        }
        __syncthreads();
        const u32 c0 = hist[2 * t], c1 = hist[2 * t + 1];
        const u32 inc = wave_incl_scan(c0 + c1);
        if (lane == 63) wsum[w] = inc;
        __syncthreads();
        u32 base = 0;
#pragma unroll
        for (int i = 0; i < 16; ++i) { const u32 s = wsum[i]; base += (i < w) ? s : 0u; }
        const u32 st0 = base + inc - c0 - c1, st1 = st0 + c0;
        hist[2 * t] = st0; hist[2 * t + 1] = st1;
        offs[((size_t)(t >> 2) * G + g) * 4 + (t & 3)] = st0 | (st1 << 16);
        u16* ids = (u16*)smem;
        __syncthreads();
#pragma unroll
        for (int i = 0; i < 4; ++i)
            if (act[i]) ids[hist[key[i]] + rank[i]] = (u16)(t * 4 + i);
        __syncthreads();
#pragma unroll
        for (int i = 0; i < 4; ++i)
            if (act[i]) {
                const u32 s = hist[key[i]], e = hist[key[i] + 1];
                u32 r = 0;
                for (u32 j = s; j < e; ++j) r += (ids[j] < (u16)(t * 4 + i)) ? 1u : 0u;
                u32x4 rec; rec[0] = (u32)s4[i]; rec[1] = rA[i]; rec[2] = rB[i]; rec[3] = rC[i];
                recs[(size_t)g * EPB + s + r] = rec;
            }
    } else if (b < G + nNodeBlocks) {
        const int nb = b - G;
        const int n = lane & 15, q = lane >> 4;
        const int node = nb * NODES_PB + w * 16 + n;
        const int nodec = min(node, N - 1);
        u32x4 sb[4];
#pragma unroll
        for (int ks = 0; ks < 4; ++ks) sb[ks] = *(const u32x4*)(sv + (size_t)nodec * 512 + ks * 32 + q * 8);
#pragma unroll
        for (int i = 0; i < 2; ++i) {
            const int u = t + i * PRE_T, k = u >> 4, cu = u & 15;
            u32x4 v = *(const u32x4*)(w1 + (size_t)u * 8);
            if ((k >> 3) & 1) { u32x4 x; x[0] = v[2]; x[1] = v[3]; x[2] = v[0]; x[3] = v[1]; v = x; }
            *(u32x4*)(smem + k * 256 + ((cu * 16) ^ ((k & 3) << 6))) = v;
        }
        __syncthreads();
        const int r = (lane & 15) >> 2, cc = lane & 3;
#pragma unroll
        for (int tt = 0; tt < 4; ++tt) {
            f32x4 acc[2];
#pragma unroll
            for (int hf = 0; hf < 2; ++hf) {
                acc[hf] = (f32x4){0.f, 0.f, 0.f, 0.f};
#pragma unroll
                for (int ks = 0; ks < 4; ++ks) {
                    const int col = (64 * tt + 16 * cc + 8 * hf) ^ (r << 6) ^ ((q & 1) << 3);
                    const int k0 = 32 * ks + 8 * q + r;
                    const u32x2 a0 = tr_read(smem + k0 * 256 + col);
                    const u32x2 a1 = tr_read(smem + (k0 + 4) * 256 + col);
                    u32x4 a; a[0] = a0[0]; a[1] = a0[1]; a[2] = a1[0]; a[3] = a1[1];
                    acc[hf] = mfma16(a, sb[ks], acc[hf]);
                }
            }
            u32x4 o;
#pragma unroll
            for (int hf = 0; hf < 2; ++hf) {
                float y[4];
#pragma unroll
                for (int i = 0; i < 4; ++i) {
                    const float x = rbf16(acc[hf][i]);
                    y[i] = x * __builtin_amdgcn_rcpf(1.0f + __expf(-x));
                }
                o[2 * hf] = pack2(y[0], y[1]); o[2 * hf + 1] = pack2(y[2], y[3]);
            }
            if (node < N) *(u32x4*)(h + (size_t)node * 128 + 32 * tt + 8 * q) = o;
        }
    } else {
        const int T = (b - G - nNodeBlocks) * PRE_T + t;
        const int fi = T >> 6;
        const int ww = fi / 12, gg = (fi % 12) >> 2, ks = fi & 3;
        const int c = lane & 15, q = lane >> 4;
        u32 v[8];
#pragma unroll
        for (int j = 0; j < 8; ++j) v[j] = w2[(size_t)(32 * ks + 8 * q + j) * 384 + 128 * gg + 16 * ww + c];
        u32x4 o;
#pragma unroll
        for (int j = 0; j < 4; ++j) o[j] = v[2 * j] | (v[2 * j + 1] << 16);
        w2f[T] = o;
    }
}

struct Res { u32x2 v[4]; };

DI void flush_rb(const f32x4 (&acc)[4], const u32x2 (&res)[4], u16* outp, bool valid) {
#pragma unroll
    for (int x = 0; x < 4; ++x) {
        u32x2 o, rr = res[x];
        asm volatile("" : "+v"(rr));
        o[0] = pack2(lo16(rr[0]) + acc[x][0], hi16(rr[0]) + acc[x][1]);
        o[1] = pack2(lo16(rr[1]) + acc[x][2], hi16(rr[1]) + acc[x][3]);
        if (valid) *(u32x2*)(outp + x * 128) = o;
    }
}

__global__ __launch_bounds__(512, 2) void main_kernel(const u16* __restrict__ sv, const u16* __restrict__ freqs,
                                                      const u16* __restrict__ wrbf, const u16* __restrict__ h,
                                                      const u32x4* __restrict__ w2f, const u32x4* __restrict__ offs,
                                                      const u32x4* __restrict__ recs, u16* __restrict__ out, int N, int NT,
                                                      int EPB) {
    extern __shared__ __attribute__((aligned(16))) char smem[];
    const int t = threadIdx.x, lane = t & 63;
    const int w = __builtin_amdgcn_readfirstlane(t >> 6);
    const int b = blockIdx.x;
    const int tile = (b & 7) * (TILES / 8) + (b >> 3);
    const int c16 = lane & 15, q = lane >> 4;
    const int node0 = tile * NT;
    const int nvalid = min(NT, N - node0);
    if (nvalid <= 0) return;

    u32x4 o = (u32x4){0u, 0u, 0u, 0u};
    if (t < G) o = offs[(size_t)tile * G + t];
    u32x4 W2[3][4];
#pragma unroll
    for (int gg = 0; gg < 3; ++gg)
#pragma unroll
        for (int ks = 0; ks < 4; ++ks) W2[gg][ks] = w2f[(w * 12 + gg * 4 + ks) * 64 + lane];
    u32x4 WR[3];
#pragma unroll
    for (int gg = 0; gg < 3; ++gg) {
        u32 v[4];
#pragma unroll
        for (int k = 0; k < 4; ++k) v[k] = wrbf[k * 384 + 128 * gg + 16 * w + c16];
        const u32 p0 = v[0] | (v[1] << 16), p1 = v[2] | (v[3] << 16);
        WR[gg][0] = q == 0 ? p0 : 0u; WR[gg][1] = q == 0 ? p1 : 0u; WR[gg][2] = WR[gg][0]; WR[gg][3] = WR[gg][1];
    }
    u32x2 res[7][4];
#pragma unroll
    for (int rb = 0; rb < 7; ++rb) {
        const int loc = min(rb * 16 + c16, nvalid - 1);
        const u16* p = sv + (size_t)(node0 + loc) * 512 + 16 * w + 4 * q;
#pragma unroll
        for (int x = 0; x < 4; ++x) res[rb][x] = *(const u32x2*)(p + x * 128);
    }
    const float fr0 = bf2f(freqs[0]), fr1 = bf2f(freqs[1]), fr2 = bf2f(freqs[2]), fr3 = bf2f(freqs[3]);

    u32* tot = (u32*)(smem + L_TOT);
    u32 rbs[8];
    u32 pre[7];
    {
        u32 st[8];
#pragma unroll
        for (int i = 0; i < 4; ++i) { st[2 * i] = o[i] & 0xffffu; st[2 * i + 1] = o[i] >> 16; }
#pragma unroll
        for (int rb = 0; rb < 7; ++rb) {
            const u32 cnt = st[rb + 1] - st[rb];
            const u32 inc = wave_incl_scan(cnt);
            pre[rb] = inc - cnt;
            if (w < 2 && lane == 63) tot[w * 8 + rb] = inc;
        }
        lds_barrier();
        rbs[0] = 0;
#pragma unroll
        for (int rb = 0; rb < 7; ++rb) {
            const u32 a = tot[rb], c = tot[8 + rb];
            if (w == 1) pre[rb] += a;
            pre[rb] += rbs[rb];
            rbs[rb + 1] = __builtin_amdgcn_readfirstlane(rbs[rb] + a + c);
        }
    }
    const int total = (int)rbs[7];
    u32 rbtab = 0;
#pragma unroll
    for (int i = 1; i < 8; ++i) rbtab = lane == i ? rbs[i] : rbtab;

    f32x4 acc[4];
#pragma unroll
    for (int x = 0; x < 4; ++x) acc[x] = (f32x4){0.f, 0.f, 0.f, 0.f};
    int cur = 0;
    u16* const outw = out + (size_t)(node0 + c16) * 512 + 16 * w + 4 * q;

    const char* dbase = (lane < 16 ? (const char*)h : (const char*)sv) + lane * 16;
    const u32 dstride = lane < 16 ? 256u : 1024u;

    u32* const l_src = (u32*)(smem + L_SRC);
    u32* const l_list = (u32*)(smem + L_LIST);
    u16* const l_dl = (u16*)(smem + L_DL);
    float* const l_rh = (float*)(smem + L_RH);
    u32x4* const l_wfa = (u32x4*)(smem + L_WFA);

#pragma unroll
    for (int gg = 0; gg < 3; ++gg) {
        asm volatile("" : "+v"(WR[gg]));
#pragma unroll
        for (int ks = 0; ks < 4; ++ks) asm volatile("" : "+v"(W2[gg][ks]));
    }
#pragma unroll
    for (int rb = 0; rb < 7; ++rb)
#pragma unroll
        for (int x = 0; x < 4; ++x) asm volatile("" : "+v"(res[rb][x]));

#define FLUSH_CUR()                                                                                        \
    do {                                                                                                   \
        const bool valid_ = cur * 16 + c16 < nvalid;                                                       \
        u16* op_ = outw + (size_t)cur * 16 * 512;                                                          \
        switch (cur) {                                                                                     \
            case 0: flush_rb(acc, res[0], op_, valid_); break;                                             \
            case 1: flush_rb(acc, res[1], op_, valid_); break;                                             \
            case 2: flush_rb(acc, res[2], op_, valid_); break;                                             \
            case 3: flush_rb(acc, res[3], op_, valid_); break;                                             \
            case 4: flush_rb(acc, res[4], op_, valid_); break;                                             \
            case 5: flush_rb(acc, res[5], op_, valid_); break;                                             \
            default: flush_rb(acc, res[6], op_, valid_); break;                                            \
        }                                                                                                  \
        for (int x_ = 0; x_ < 4; ++x_) acc[x_] = (f32x4){0.f, 0.f, 0.f, 0.f};                              \
        ++cur;                                                                                             \
    } while (0)

    for (int W0 = 0; W0 < total; W0 += CAP) {
        const int nrec = min(CAP, total - W0);
        const int nch = (nrec + 31) >> 5;
        if (W0 > 0) lds_barrier();
        if (t < G) {
#pragma unroll
            for (int rb = 0; rb < 7; ++rb) {
                const u32 s0 = (o[rb >> 1] >> (16 * (rb & 1))) & 0xffffu;
                const u32 s1 = (o[(rb + 1) >> 1] >> (16 * ((rb + 1) & 1))) & 0xffffu;
                const int P = (int)pre[rb] - W0;
                const int lo = max(0, -P), hi = min((int)(s1 - s0), CAP - P);
#pragma clang loop vectorize(disable) unroll(disable)
                for (int i = lo; i < hi; ++i) l_list[P + i] = (u32)(t * EPB) + s0 + (u32)i;
            }
        }
        lds_barrier();
        u32x4 rec = (u32x4){0u, 0xffffu, 0u, 0u};
        const bool have = t < nrec;
        if (have) rec = recs[l_list[t]];
        if (t < nch * 32) l_src[t] = rec[0];
        lds_barrier();
#pragma unroll
        for (int pc = 0; pc < 2; ++pc) {
            if (pc < nch) {
#pragma unroll
                for (int i = 0; i < 4; ++i) {
                    const u32 s = __builtin_amdgcn_readfirstlane(l_src[pc * 32 + 4 * w + i]);
                    dma16(dbase + (size_t)s * dstride, smem + L_BUF + pc * CHUNK_B + (4 * w + i) * PITCH);
                }
            }
        }
        if (t < nch * 32) {
            const float d = hi16(rec[1]);
            u32x4 a = (u32x4){0u, 0u, 0u, 0u};
            float rh0 = 0.f, rh1 = 0.f, rh2 = 0.f;
            if (have) {
                const float inv = __builtin_amdgcn_rcpf(d);
                rh0 = rbf16(lo16(rec[2]) * inv); rh1 = rbf16(hi16(rec[2]) * inv); rh2 = rbf16(lo16(rec[3]) * inv);
                const float i2pi = 0.15915494309189535f;
                float ang = rbf16(rbf16(3.14159265358979323846f * d) / 5.0f);
                float fc = rbf16(__builtin_amdgcn_cosf(__builtin_amdgcn_fractf(ang * i2pi)));
                fc = rbf16(0.5f * rbf16(fc + 1.0f));
                fc = d < 5.0f ? fc : 0.0f * d;
                float cf[4];
                const float fr[4] = {fr0, fr1, fr2, fr3};
#pragma unroll
                for (int k = 0; k < 4; ++k) {
                    const float arg = rbf16(d * fr[k]);
                    const float sn = rbf16(__builtin_amdgcn_sinf(__builtin_amdgcn_fractf(arg * i2pi)));
                    cf[k] = rbf16(sn * inv) * fc;
                }
                float hi[4], lo[4];
#pragma unroll
                for (int k = 0; k < 4; ++k) { hi[k] = rbf16(cf[k]); lo[k] = cf[k] - hi[k]; }
                a[0] = pack2(hi[0], hi[1]); a[1] = pack2(hi[2], hi[3]); a[2] = pack2(lo[0], lo[1]); a[3] = pack2(lo[2], lo[3]);
            }
            l_wfa[t] = a;
            l_rh[t] = rh0; l_rh[CAP + t] = rh1; l_rh[2 * CAP + t] = rh2;
            l_dl[t] = (u16)(rec[1] & 0xffffu);
        }

        for (int ch = 0; ch < nch; ++ch) {
            if (ch + 1 < nch) asm volatile("s_waitcnt vmcnt(4)" ::: "memory");
            else asm volatile("s_waitcnt vmcnt(0)" ::: "memory");
            lds_barrier();
            if (ch + 2 < nch) {
                const int pb = (ch + 2) % NBUF;
                const u32x4 s4 = *(const u32x4*)(l_src + (ch + 2) * 32 + 4 * w);
#pragma unroll
                for (int i = 0; i < 4; ++i) {
                    const u32 s = __builtin_amdgcn_readfirstlane(s4[i]);
                    dma16(dbase + (size_t)s * dstride, smem + L_BUF + pb * CHUNK_B + (4 * w + i) * PITCH);
                }
            }
            const char* B = smem + L_BUF + (ch % NBUF) * CHUNK_B;
            const int eb = ch * 32;
            u32x4 A[4];
#pragma unroll
            for (int blk = 0; blk < 2; ++blk) {
                f32x4 Z[3], Wf[3];
                const u32x4 wa = l_wfa[eb + 16 * blk + c16];
#pragma unroll
                for (int gg = 0; gg < 3; ++gg) {
                    Z[gg] = (f32x4){0.f, 0.f, 0.f, 0.f};
                    Wf[gg] = mfma16(wa, WR[gg], (f32x4){0.f, 0.f, 0.f, 0.f});
                }
#pragma unroll
                for (int ks = 0; ks < 4; ++ks) {
                    const u32x4 a = *(const u32x4*)(B + (16 * blk + c16) * PITCH + ks * 64 + q * 16);
#pragma unroll
                    for (int gg = 0; gg < 3; ++gg) Z[gg] = mfma16(a, W2[gg][ks], Z[gg]);
                }
                float mvv[4], mvr[4], ms[4];
#pragma unroll
                for (int r = 0; r < 4; ++r) { ms[r] = Z[0][r] * Wf[0][r]; mvv[r] = Z[1][r] * Wf[1][r]; mvr[r] = Z[2][r] * Wf[2][r]; }
                A[0][2 * blk] = pack2(ms[0], ms[1]); A[0][2 * blk + 1] = pack2(ms[2], ms[3]);
#pragma unroll
                for (int x = 0; x < 3; ++x) {
                    const u32x2 vv = tr_read(B + (16 * blk + 4 * q + (c16 >> 2)) * PITCH + 256 * (1 + x) + 32 * w + 8 * (c16 & 3));
                    const f32x4 rh = *(const f32x4*)(l_rh + x * CAP + eb + 16 * blk + 4 * q);
                    const float u0 = mvv[0] * lo16(vv[0]) + mvr[0] * rh[0];
                    const float u1 = mvv[1] * hi16(vv[0]) + mvr[1] * rh[1];
                    const float u2 = mvv[2] * lo16(vv[1]) + mvr[2] * rh[2];
                    const float u3 = mvv[3] * hi16(vv[1]) + mvr[3] * rh[3];
                    A[1 + x][2 * blk] = pack2(u0, u1); A[1 + x][2 * blk + 1] = pack2(u2, u3);
                }
            }
            const u32x2 dA = *(const u32x2*)(l_dl + eb + 4 * q), dB = *(const u32x2*)(l_dl + eb + 16 + 4 * q);
            const int gpos0 = W0 + eb, gpos1 = W0 + min(eb + 32, nrec);
            while (true) {
                const u32 lo_ = __builtin_amdgcn_readlane(rbtab, cur), hi_ = __builtin_amdgcn_readlane(rbtab, cur + 1);
                if ((int)hi_ > gpos0 && (int)lo_ < gpos1) {
                    const u32 tg = (u32)(cur * 16 + c16) * 0x10001u;
                    u32x4 S;
                    const u32 dd[4] = {dA[0], dA[1], dB[0], dB[1]};
#pragma unroll
                    for (int i = 0; i < 4; ++i) {
                        u16x2 xx = __builtin_bit_cast(u16x2, dd[i] ^ tg);
                        u16x2 y = __builtin_elementwise_min(xx, (u16x2){1, 1});
                        u16x2 z = y * (u16x2){0xC080, 0xC080} + (u16x2){0x3F80, 0x3F80};
                        S[i] = __builtin_bit_cast(u32, z);
                    }
#pragma unroll
                    for (int x = 0; x < 4; ++x) acc[x] = mfma16(A[x], S, acc[x]);
                }
                if ((int)hi_ > gpos1 || cur == 6) break;
                if ((int)hi_ == gpos1 && gpos1 == total) break;
                FLUSH_CUR();
            }
        }
    }
    while (cur < 7) FLUSH_CUR();
#undef FLUSH_CUR
}

extern "C" void kernel_launch(void* const* d_in, const int* in_sizes, int n_in, void* d_out, int out_size, void* d_ws,
                              size_t ws_size, hipStream_t stream) {
    const u16* sv = (const u16*)d_in[0];
    const u16* pos = (const u16*)d_in[1];
    const int* src = (const int*)d_in[2];
    const int* dst = (const int*)d_in[3];
    const u16* freqs = (const u16*)d_in[4];
    const u16* w1 = (const u16*)d_in[5];
    const u16* w2 = (const u16*)d_in[6];
    const u16* wrbf = (const u16*)d_in[7];
    u16* out = (u16*)d_out;
    const int N = in_sizes[0] / 512;
    const int E = in_sizes[2];
    const int NT = (N + TILES - 1) / TILES;
    const int EPB = ((E + G - 1) / G + 3) / 4 * 4;
    const u32 ntmagic = (u32)((0x100000000ull + NT - 1) / NT);
    const int nNodeBlocks = (N + NODES_PB - 1) / NODES_PB;

    char* ws = (char*)d_ws;
    size_t off = 0;
    u16* h = (u16*)(ws + off); off += ((size_t)N * 256 + 255) / 256 * 256;
    u32x4* w2f = (u32x4*)(ws + off); off += 6144 * 16;
    u32* offs = (u32*)(ws + off); off += (size_t)TILES * G * 16;
    u32x4* recs = (u32x4*)(ws + off);

    const int posBytes = ((N * 6 + 15) / 16) * 16;
    const int preLds = posBytes + 2048 * 4 + 64;
    (void)hipFuncSetAttribute((const void*)pre_kernel, hipFuncAttributeMaxDynamicSharedMemorySize, preLds);
    (void)hipFuncSetAttribute((const void*)main_kernel, hipFuncAttributeMaxDynamicSharedMemorySize, MAIN_LDS);
    pre_kernel<<<G + nNodeBlocks + W2_BLOCKS, PRE_T, preLds, stream>>>(sv, pos, src, dst, w1, w2, h, w2f, offs, recs, N, E, EPB,
                                                                       NT, ntmagic, nNodeBlocks);
    main_kernel<<<TILES, 512, MAIN_LDS, stream>>>(sv, freqs, wrbf, h, w2f, (const u32x4*)offs, recs, out, N, NT, EPB);
}
```

```cpp
#include <hip/hip_runtime.h>
#include <stdint.h>

typedef unsigned short u16;
typedef unsigned int u32;
typedef unsigned long long u64;
typedef __attribute__((ext_vector_type(8))) __bf16 bf16x8;
typedef __attribute__((ext_vector_type(2))) __bf16 bf16x2;
typedef __attribute__((ext_vector_type(4))) float f32x4;
typedef __attribute__((ext_vector_type(4))) u32 u32x4;
typedef __attribute__((ext_vector_type(2))) u32 u32x2;
typedef __attribute__((ext_vector_type(4))) int i32x4;
typedef __attribute__((ext_vector_type(4))) short s16x4;
typedef __attribute__((ext_vector_type(2))) u16 u16x2;
typedef __attribute__((address_space(3))) char lds_char;

#define DI __device__ __forceinline__

constexpr int TILES = 256;
constexpr int G = 128;
constexpr int PRE_T = 1024;
constexpr int NODES_PB = 256;
constexpr int W2_BLOCKS = 6;
constexpr int CAP = 512;
constexpr int RC = 2048;
constexpr int PITCH = 1056;
constexpr int CHUNK_B = 32 * PITCH;
constexpr int SPITCH = 1040;
constexpr int STAGE_B = 16 * SPITCH;

constexpr int L_BUF = 0;
constexpr int L_STG = 3 * CHUNK_B;
constexpr int L_WFA = L_STG + 2 * STAGE_B;
constexpr int L_RH = L_WFA + CAP * 16;
constexpr int L_DL = L_RH + 3 * CAP * 4;
constexpr int L_FIN = L_DL + CAP * 2;
constexpr int L_HIST = L_FIN + RC * 4;
constexpr int L_START = L_HIST + 512;
constexpr int L_POSD = L_START + 544;
constexpr int L_MISC = L_POSD + 1344;
constexpr int MAIN_LDS = L_MISC + 64;
static_assert(MAIN_LDS <= 163840, "lds");

DI float bf2f(u32 b) { return __uint_as_float(b << 16); }
DI float rbf16(float x) { return (float)(__bf16)x; }
DI u32 pack2(float a, float b) { bf16x2 v; v[0] = (__bf16)a; v[1] = (__bf16)b; return __builtin_bit_cast(u32, v); }
DI float lo16(u32 p) { return __uint_as_float(p << 16); }
DI float hi16(u32 p) { return __uint_as_float(p & 0xffff0000u); }

DI u32 wave_incl_scan(u32 v) {
    v += (u32)__builtin_amdgcn_update_dpp(0, (int)v, 0x111, 0xf, 0xf, false);
    v += (u32)__builtin_amdgcn_update_dpp(0, (int)v, 0x112, 0xf, 0xf, false);
    v += (u32)__builtin_amdgcn_update_dpp(0, (int)v, 0x114, 0xf, 0xf, false);
    v += (u32)__builtin_amdgcn_update_dpp(0, (int)v, 0x118, 0xf, 0xf, false);
    v += (u32)__builtin_amdgcn_update_dpp(0, (int)v, 0x142, 0xa, 0xf, false);
    v += (u32)__builtin_amdgcn_update_dpp(0, (int)v, 0x143, 0xc, 0xf, false);
    return v;
}

DI void dma16(const void* gsrc, u32 lds_base) {
    asm volatile("s_mov_b32 m0, %1\n\ts_nop 0\n\tglobal_load_lds_dwordx4 %0, off" ::"v"(gsrc), "s"(lds_base) : "memory");
}
DI void lds_barrier() {
    asm volatile("s_waitcnt lgkmcnt(0)" ::: "memory");
    __builtin_amdgcn_s_barrier();
    asm volatile("" ::: "memory");
}
#define VMWAIT(n) asm volatile("s_waitcnt vmcnt(" #n ")" ::: "memory")
DI void wait_vm(int n) {
    if (n >= 8) {
        if (n >= 12) { if (n >= 16) VMWAIT(16); else if (n >= 14) VMWAIT(14); else VMWAIT(12); }
        else { if (n >= 10) VMWAIT(10); else VMWAIT(8); }
    } else if (n >= 4) {
        if (n >= 6) VMWAIT(6); else VMWAIT(4);
    } else {
        if (n >= 2) VMWAIT(2); else VMWAIT(0);
    }
}
typedef __attribute__((ext_vector_type(4))) short v4i16_t;
DI u32x2 tr_read(const char* p) {
    v4i16_t r = __builtin_amdgcn_ds_read_tr16_b64_v4i16((__attribute__((address_space(3))) v4i16_t*)p);
    return __builtin_bit_cast(u32x2, r);
}
DI f32x4 mfma16(u32x4 a, u32x4 b, f32x4 c) {
    return __builtin_amdgcn_mfma_f32_16x16x32_bf16(__builtin_bit_cast(bf16x8, a), __builtin_bit_cast(bf16x8, b), c, 0, 0, 0);
}
DI f32x4 mfma16k16(u32x2 a, u32x2 b, f32x4 c) {
    return __builtin_amdgcn_mfma_f32_16x16x16bf16_1k(__builtin_bit_cast(s16x4, a), __builtin_bit_cast(s16x4, b), c, 0, 0, 0);
}
DI void aload16(u32x4& d, const void* base, u32 off) {
    asm volatile("global_load_dwordx4 %0, %1, %2" : "=v"(d) : "v"(off), "s"(base) : "memory");
}
DI void aload8(u32x2& d, const void* base, u32 off) {
    asm volatile("global_load_dwordx2 %0, %1, %2" : "=v"(d) : "v"(off), "s"(base) : "memory");
}
DI void aload2(u32& d, const void* base, u32 off) {
    asm volatile("global_load_ushort %0, %1, %2" : "=v"(d) : "v"(off), "s"(base) : "memory");
}

__global__ __launch_bounds__(PRE_T) void pre_kernel(const u16* __restrict__ sv, const u16* __restrict__ pos,
                                                    const int* __restrict__ src, const int* __restrict__ dst,
                                                    const u16* __restrict__ w1, const u16* __restrict__ w2,
                                                    const u16* __restrict__ wrbf, u16* __restrict__ h,
                                                    u32x4* __restrict__ w2f, u32x2* __restrict__ wrf, u32* __restrict__ bins,
                                                    int N, int E, int EPB, int NT, u32 ntmagic, int nNodeBlocks) {
    extern __shared__ __attribute__((aligned(16))) char smem[];
    const int t = threadIdx.x, lane = t & 63;
    const int w = __builtin_amdgcn_readfirstlane(t >> 6);
    const int b = blockIdx.x;
    if (b < G) {
        const int g = (b & 7) * (G / 8) + (b >> 3);
        const int posUnits = (N * 6 + 15) >> 4;
        const int posBytes = posUnits * 16;
        u32* hist = (u32*)(smem + posBytes);
        const int e0 = g * EPB + t * 4;
        const int eEnd = min(E, (g + 1) * EPB);
        const int e0c = min(e0, E - 4);
        u32x4 s4, d4;
        aload16(s4, src, (u32)e0c * 4u);
        aload16(d4, dst, (u32)e0c * 4u);
        const u32 lbase = (u32)(size_t)(lds_char*)smem;
        for (int u0 = w * 64; u0 < posUnits; u0 += PRE_T) {
            const int u = u0 + lane;
            if ((u + 1) * 16 <= N * 6) dma16((const char*)pos + (size_t)u * 16, lbase + (u32)u0 * 16u);
        }
        if (t < TILES) hist[t] = 0;
        {
            const int tail0 = (N * 6) & ~15;
            if (t < ((N * 6 - tail0) >> 1)) ((u16*)(smem + tail0))[t] = pos[(tail0 >> 1) + t];
        }
        VMWAIT(0);
        asm volatile("" : "+v"(s4), "+v"(d4));
        lds_barrier();
        const u32* posw = (const u32*)smem;
#pragma unroll
        for (int i = 0; i < 4; ++i) {
            const bool ok = e0c + i >= e0 && e0c + i < eEnd;
            const u32 si = s4[i], di = d4[i];
            const u32 as = (3 * si) >> 1, ad = (3 * di) >> 1;
            const u32 as0 = posw[as], as1 = posw[as + 1];
            const u32 ad0 = posw[ad], ad1 = posw[ad + 1];
            const u32 shs = (si & 1) * 16, shd = (di & 1) * 16;
            const u32 sxy = __builtin_amdgcn_alignbit(as1, as0, shs), dxy = __builtin_amdgcn_alignbit(ad1, ad0, shd);
            const float rx = lo16(sxy) - lo16(dxy);
            const float ry = hi16(sxy) - hi16(dxy);
            const float rz = lo16(as1 >> shs) - lo16(ad1 >> shd);
            const float ss = rx * rx + ry * ry + rz * rz;
            const bool drop = (__float_as_uint(ss) - 0x41CA0000u) <= (0x7E000000u - 0x41CA0000u);
            if (ok && !drop) {
                const u32 tile = __umulhi(di, ntmagic);
                const u32 loc = di - tile * (u32)NT;
                const u32 rank = atomicAdd(&hist[tile], 1u);
                if (rank < 15u) bins[((size_t)tile * G + g) * 16 + 1 + rank] = (loc << 24) | si;
            }
        }
        lds_barrier();
        if (t < TILES) bins[((size_t)t * G + g) * 16] = hist[t];
    } else if (b < G + nNodeBlocks) {
        const int nb = b - G;
        const int n = lane & 15, q = lane >> 4;
        const int node = nb * NODES_PB + w * 16 + n;
        const int nodec = min(node, N - 1);
        u32x4 sb[4];
#pragma unroll
        for (int ks = 0; ks < 4; ++ks) sb[ks] = *(const u32x4*)(sv + (size_t)nodec * 512 + ks * 32 + q * 8);
#pragma unroll
        for (int i = 0; i < 2; ++i) {
            const int u = t + i * PRE_T, k = u >> 4, cu = u & 15;
            u32x4 v = *(const u32x4*)(w1 + (size_t)u * 8);
            if ((k >> 3) & 1) { u32x4 x; x[0] = v[2]; x[1] = v[3]; x[2] = v[0]; x[3] = v[1]; v = x; }
            *(u32x4*)(smem + k * 256 + ((cu * 16) ^ ((k & 3) << 6))) = v;
        }
        __syncthreads();
        const int r = (lane & 15) >> 2, cc = lane & 3;
#pragma unroll
        for (int tt = 0; tt < 4; ++tt) {
            f32x4 acc[2];
#pragma unroll
            for (int hf = 0; hf < 2; ++hf) {
                acc[hf] = (f32x4){0.f, 0.f, 0.f, 0.f};
#pragma unroll
                for (int ks = 0; ks < 4; ++ks) {
                    const int col = (64 * tt + 16 * cc + 8 * hf) ^ (r << 6) ^ ((q & 1) << 3);
                    const int k0 = 32 * ks + 8 * q + r;
                    const u32x2 a0 = tr_read(smem + k0 * 256 + col);
                    const u32x2 a1 = tr_read(smem + (k0 + 4) * 256 + col);
                    u32x4 a; a[0] = a0[0]; a[1] = a0[1]; a[2] = a1[0]; a[3] = a1[1];
                    acc[hf] = mfma16(a, sb[ks], acc[hf]);
                }
            }
            u32x4 o;
#pragma unroll
            for (int hf = 0; hf < 2; ++hf) {
                float y[4];
#pragma unroll
                for (int i = 0; i < 4; ++i) {
                    const float x = rbf16(acc[hf][i]);
                    y[i] = x * __builtin_amdgcn_rcpf(1.0f + __expf(-x));
                }
                o[2 * hf] = pack2(y[0], y[1]); o[2 * hf + 1] = pack2(y[2], y[3]);
            }
            if (node < N) *(u32x4*)(h + (size_t)node * 128 + 32 * tt + 8 * q) = o;
        }
    } else {
        const int T = (b - G - nNodeBlocks) * PRE_T + t;
        const int fi = T >> 6;
        const int ww = fi / 12, gg = (fi % 12) >> 2, ks = fi & 3;
        const int c = lane & 15, q = lane >> 4;
        u32 v[8];
#pragma unroll
        for (int j = 0; j < 8; ++j) v[j] = w2[(size_t)(32 * ks + 8 * q + j) * 384 + 128 * gg + 16 * ww + c];
        u32x4 o;
#pragma unroll
        for (int j = 0; j < 4; ++j) o[j] = v[2 * j] | (v[2 * j + 1] << 16);
        w2f[T] = o;
        if (ks == 0) {
            u32 r[4];
#pragma unroll
            for (int k = 0; k < 4; ++k) r[k] = wrbf[k * 384 + 128 * gg + 16 * ww + c];
            u32x2 p; p[0] = q < 2 ? (r[0] | (r[1] << 16)) : 0u; p[1] = q < 2 ? (r[2] | (r[3] << 16)) : 0u;
            wrf[(ww * 3 + gg) * 64 + lane] = p;
        }
    }
}

__global__ __launch_bounds__(512, 2) void main_kernel(const u16* __restrict__ sv, const u16* __restrict__ pos,
                                                      const int* __restrict__ src, const int* __restrict__ dst,
                                                      const u16* __restrict__ freqs, const u16* __restrict__ h,
                                                      const u32x4* __restrict__ w2f, const u32x2* __restrict__ wrf,
                                                      const u32* __restrict__ bins, u16* __restrict__ out, int N, int E,
                                                      int NT) {
    extern __shared__ __attribute__((aligned(16))) char smem[];
    const int t = threadIdx.x, lane = t & 63;
    const int w = __builtin_amdgcn_readfirstlane(t >> 6);
    const int b = blockIdx.x;
    const int tile = (b & 7) * (TILES / 8) + (b >> 3);
    const int c16 = lane & 15, q = lane >> 4;
    const int node0 = tile * NT;
    const int nvalid = min(NT, N - node0);
    if (nvalid <= 0) return;
    const u32 lbase = (u32)(size_t)(lds_char*)smem;

    u32x4 bw;
    aload16(bw, (const char*)bins + (size_t)tile * (G * 64), (u32)t * 16u);
    u32 pdv;
    aload2(pdv, pos + (size_t)node0 * 3, (u32)min(t, 3 * nvalid - 1) * 2u);
    u32x4 W2[3][4];
#pragma unroll
    for (int gg = 0; gg < 3; ++gg)
#pragma unroll
        for (int ks = 0; ks < 4; ++ks) aload16(W2[gg][ks], w2f, (u32)((w * 12 + gg * 4 + ks) * 64 + lane) * 16u);
    u32x2 WR[3];
#pragma unroll
    for (int gg = 0; gg < 3; ++gg) aload8(WR[gg], wrf, (u32)((w * 3 + gg) * 64 + lane) * 8u);
    u32x4 res[7][2];
    {
        const char* rbase = (const char*)sv + (size_t)node0 * 1024;
#pragma unroll
        for (int rb = 0; rb < 7; ++rb)
#pragma unroll
            for (int i = 0; i < 2; ++i)
                aload16(res[rb][i], rbase, (u32)min(rb * 16 + 2 * w + i, nvalid - 1) * 1024u + (u32)lane * 16u);
    }
    const float fr0 = bf2f(freqs[0]), fr1 = bf2f(freqs[1]), fr2 = bf2f(freqs[2]), fr3 = bf2f(freqs[3]);

    u32* const l_tmp = (u32*)(smem + L_STG);
    u32* const l_fin = (u32*)(smem + L_FIN);
    u32* const l_hist = (u32*)(smem + L_HIST);
    u32* const l_start = (u32*)(smem + L_START);
    float* const l_posd = (float*)(smem + L_POSD);
    u32* const l_misc = (u32*)(smem + L_MISC);
    u16* const l_dl = (u16*)(smem + L_DL);
    float* const l_rh = (float*)(smem + L_RH);

    if (t < 128) l_hist[t] = 0;
    if (t == 128) l_misc[0] = 0;
    lds_barrier();

    VMWAIT(29);
    asm volatile("" : "+v"(bw), "+v"(pdv));
    if (t < 3 * nvalid) l_posd[t] = bf2f(pdv);
    const u32 cnt = (u32)__builtin_amdgcn_update_dpp(0, (int)bw[0], 0x00, 0xf, 0xf, false);
    if (cnt > 15u) l_misc[0] = 1;
    u32 rk[4];
    bool vl[4];
#pragma unroll
    for (int i = 0; i < 4; ++i) {
        const u32 widx = 4 * (t & 3) + i;
        vl[i] = widx >= 1 && widx <= cnt && cnt <= 15u;
        rk[i] = 0;
        if (vl[i]) rk[i] = atomicAdd(&l_hist[bw[i] >> 24], 1u);
    }
    lds_barrier();
    u32 starts;
    int total;
    {
        const u32x2 hh = *(const u32x2*)(l_hist + 2 * lane);
        const u32 inc = wave_incl_scan(hh[0] + hh[1]);
        const u32 s0 = inc - hh[0] - hh[1], s1 = s0 + hh[0];
        starts = s0 | (s1 << 16);
        total = __builtin_amdgcn_readlane((int)inc, 63);
        if (w == 0) { u32x2 sx; sx[0] = s0; sx[1] = s1; *(u32x2*)(l_start + 2 * lane) = sx; if (lane == 63) l_start[128] = inc; }
    }
    const bool slow = __builtin_amdgcn_readfirstlane((int)l_misc[0]) != 0;
#pragma unroll
    for (int i = 0; i < 4; ++i) {
        const u32 loc = (bw[i] >> 24) & 127u;
        const u32 p = (u32)__builtin_amdgcn_ds_bpermute((int)((loc >> 1) << 2), (int)starts);
        const u32 st = (loc & 1) ? (p >> 16) : (p & 0xffffu);
        if (vl[i]) l_tmp[st + rk[i]] = bw[i];
    }
    lds_barrier();
    for (int p = t; p < total; p += 512) {
        const u32 rec = l_tmp[p];
        const u32 loc = rec >> 24;
        const int s = (int)l_start[loc], e = (int)l_start[loc + 1];
        int rank = 0;
        for (int j = s; j < e; j += 2) {
            const u32 k0 = l_tmp[j], k1 = l_tmp[j + 1];
            rank += (k0 < rec || (k0 == rec && j < p)) ? 1 : 0;
            rank += (j + 1 < e && (k1 < rec || (k1 == rec && j + 1 < p))) ? 1 : 0;
        }
        l_fin[s + rank] = rec;
    }
    lds_barrier();

    f32x4 acc[4];
#pragma unroll
    for (int x = 0; x < 4; ++x) acc[x] = (f32x4){0.f, 0.f, 0.f, 0.f};
    int cur = 0;
    int sto = 0;
    int epoch = 0, ePrev1 = -2, ePrev2 = -2;
    int issued = 0;

    const char* dbase = (lane < 16 ? (const char*)h : (const char*)sv) + lane * 16;
    const u32 dstride = lane < 16 ? 256u : 1024u;
    u16* const outw = out + (size_t)(node0 + 2 * w) * 512 + lane * 8;

#define STORE_PENDING(lim)                                                                                  \
    while (sto < (lim)) {                                                                                   \
        const char* sp_ = smem + L_STG + (sto & 1) * STAGE_B + 2 * w * SPITCH + lane * 16;                  \
        const u32x4 d0_ = *(const u32x4*)sp_, d1_ = *(const u32x4*)(sp_ + SPITCH);                          \
        u32x4 r0_, r1_;                                                                                     \
        switch (sto) {                                                                                      \
            case 0: r0_ = res[0][0]; r1_ = res[0][1]; break;                                                \
            case 1: r0_ = res[1][0]; r1_ = res[1][1]; break;                                                \
            case 2: r0_ = res[2][0]; r1_ = res[2][1]; break;                                                \
            case 3: r0_ = res[3][0]; r1_ = res[3][1]; break;                                                \
            case 4: r0_ = res[4][0]; r1_ = res[4][1]; break;                                                \
            case 5: r0_ = res[5][0]; r1_ = res[5][1]; break;                                                \
            default: r0_ = res[6][0]; r1_ = res[6][1]; break;                                               \
        }                                                                                                   \
        u32x4 o0_, o1_;                                                                                     \
        _Pragma("unroll") for (int i_ = 0; i_ < 4; ++i_) {                                                  \
            o0_[i_] = pack2(lo16(r0_[i_]) + lo16(d0_[i_]), hi16(r0_[i_]) + hi16(d0_[i_]));                  \
            o1_[i_] = pack2(lo16(r1_[i_]) + lo16(d1_[i_]), hi16(r1_[i_]) + hi16(d1_[i_]));                  \
        }                                                                                                   \
        const int row_ = sto * 16 + 2 * w;                                                                  \
        u16* op_ = outw + (size_t)sto * 16 * 512;                                                           \
        if (row_ < nvalid) { __builtin_nontemporal_store(o0_, (u32x4*)op_); ++issued; }                     \
        if (row_ + 1 < nvalid) { __builtin_nontemporal_store(o1_, (u32x4*)(op_ + 512)); ++issued; }         \
        ++sto;                                                                                              \
    }
#define EPOCH_BARRIER()                                                                                     \
    do {                                                                                                    \
        const int lim_ = cur;                                                                               \
        lds_barrier();                                                                                      \
        ++epoch;                                                                                            \
        STORE_PENDING(lim_);                                                                                \
    } while (0)
#define FLUSH_CUR()                                                                                         \
    do {                                                                                                    \
        while (epoch < ePrev2 + 2) EPOCH_BARRIER();                                                         \
        char* sp_ = smem + L_STG + (cur & 1) * STAGE_B + c16 * SPITCH + 32 * w + 8 * q;                     \
        _Pragma("unroll") for (int x_ = 0; x_ < 4; ++x_) {                                                  \
            u32x2 o_; o_[0] = pack2(acc[x_][0], acc[x_][1]); o_[1] = pack2(acc[x_][2], acc[x_][3]);         \
            *(u32x2*)(sp_ + 256 * x_) = o_;                                                                 \
            acc[x_] = (f32x4){0.f, 0.f, 0.f, 0.f};                                                          \
        }                                                                                                   \
        ePrev2 = ePrev1; ePrev1 = epoch;                                                                    \
        ++cur;                                                                                              \
    } while (0)

    int W0 = 0;
    int slowR = 0, slowE = 0;
    bool first = true;
    for (;;) {
        int nrec;
        u32 rbtab;
        const u32* list;
        if (!slow) {
            if (W0 >= total) break;
            nrec = min(CAP, total - W0);
            list = l_fin + W0;
            const u32 nx = (u32)__builtin_amdgcn_ds_bpermute(((lane & 7) + 1) * 32, (int)starts) & 0xffffu;
            rbtab = (u32)min(max((int)nx - W0, 0), nrec);
            W0 += CAP;
            if (!first) lds_barrier();
        } else {
            int n = 0;
            lds_barrier();
            while (slowR < 7) {
                if (slowE >= E) {
                    ++slowR; slowE = 0;
                    if (n > 0) { --slowR; slowE = E; break; }
                    continue;
                }
                const int e = slowE + t;
                int di = -1, si = 0;
                if (e < E) { di = dst[e]; si = src[e]; }
                asm volatile("" : "+v"(di), "+v"(si));
                const int loc = di - node0;
                const bool m = e < E && loc >= 0 && loc < nvalid && (loc >> 4) == slowR;
                const u64 bal = __ballot(m);
                const int pw = __builtin_amdgcn_mbcnt_hi((u32)(bal >> 32), __builtin_amdgcn_mbcnt_lo((u32)bal, 0));
                if (lane == 0) l_hist[w] = (u32)__popcll(bal);
                lds_barrier();
                int basep = 0, all = 0;
#pragma unroll
                for (int i = 0; i < 8; ++i) { const int c = (int)l_hist[i]; basep += i < w ? c : 0; all += c; }
                all = __builtin_amdgcn_readfirstlane(all);
                const bool fits = n + all <= CAP;
                if (fits && m) l_fin[n + basep + pw] = ((u32)loc << 24) | (u32)si;
                lds_barrier();
                if (!fits) break;
                n += all; slowE += 512;
            }
            if (n == 0) {
                if (slowR >= 7) break;
            }
            nrec = n;
            list = l_fin;
            rbtab = lane < slowR ? 0u : (u32)n;
            if (slowE >= E) { ++slowR; slowE = 0; }
            if (n == 0) continue;
        }
        const int nch = (nrec + 31) >> 5;
        const int p = (lane >> 2) * 32 + 4 * w + (lane & 3);
        const u32 rec = list[min(p, nrec - 1)];
        const u32 rsrc = rec & 0xffffffu;
        u32 px, py, pz;
        aload2(px, pos, rsrc * 6u);
        aload2(py, pos + 1, rsrc * 6u);
        aload2(pz, pos + 2, rsrc * 6u);
        issued += 3;
        const int mPos = issued;
        int mA, mB, mC;
        u32 bA = lbase + L_BUF, bB = lbase + L_BUF + CHUNK_B, bC = lbase + L_BUF + 2 * CHUNK_B;
#define DMA_CHUNK(c, buf)                                                                                   \
    do {                                                                                                    \
        _Pragma("unroll") for (int i_ = 0; i_ < 4; ++i_) {                                                  \
            const u32 s_ = (u32)__builtin_amdgcn_readlane((int)rsrc, (c) * 4 + i_);                         \
            dma16(dbase + (size_t)s_ * dstride, (buf) + (u32)(4 * w + i_) * PITCH);                         \
        }                                                                                                   \
        issued += 4;                                                                                        \
    } while (0)
        DMA_CHUNK(0, bA);
        mA = issued;
        if (nch > 1) DMA_CHUNK(1, bB);
        mB = issued;
        if (nch > 2) DMA_CHUNK(2, bC);
        mC = issued;
        wait_vm(issued - mPos);
        asm volatile("" : "+v"(px), "+v"(py), "+v"(pz));
        if (first) {
#pragma unroll
            for (int gg = 0; gg < 3; ++gg) {
                asm volatile("" : "+v"(WR[gg]));
#pragma unroll
                for (int ks = 0; ks < 4; ++ks) asm volatile("" : "+v"(W2[gg][ks]));
            }
#pragma unroll
            for (int rb = 0; rb < 7; ++rb) asm volatile("" : "+v"(res[rb][0]), "+v"(res[rb][1]));
            first = false;
        }
        if (p < nch * 32) {
            u32x4 a = (u32x4){0u, 0u, 0u, 0u};
            float rh0 = 0.f, rh1 = 0.f, rh2 = 0.f;
            const u32 loc = rec >> 24;
            if (p < nrec) {
                const float rx = rbf16(bf2f(px) - l_posd[3 * loc]);
                const float ry = rbf16(bf2f(py) - l_posd[3 * loc + 1]);
                const float rz = rbf16(bf2f(pz) - l_posd[3 * loc + 2]);
                const float ss = rbf16(rbf16(rx * rx) + rbf16(ry * ry) + rbf16(rz * rz));
                const float d = rbf16(sqrtf(rbf16(ss + 1e-8f)));
                const float inv = __builtin_amdgcn_rcpf(d);
                rh0 = rbf16(rx * inv); rh1 = rbf16(ry * inv); rh2 = rbf16(rz * inv);
                const float i2pi = 0.15915494309189535f;
                float ang = rbf16(rbf16(3.14159265358979323846f * d) / 5.0f);
                float fc = rbf16(__builtin_amdgcn_cosf(__builtin_amdgcn_fractf(ang * i2pi)));
                fc = rbf16(0.5f * rbf16(fc + 1.0f));
                fc = d < 5.0f ? fc : 0.0f * d;
                float cf[4];
                const float fr[4] = {fr0, fr1, fr2, fr3};
#pragma unroll
                for (int k = 0; k < 4; ++k) {
                    const float arg = rbf16(d * fr[k]);
                    const float sn = rbf16(__builtin_amdgcn_sinf(__builtin_amdgcn_fractf(arg * i2pi)));
                    cf[k] = rbf16(sn * inv) * fc;
                }
                float hi[4], lo[4];
#pragma unroll
                for (int k = 0; k < 4; ++k) { hi[k] = rbf16(cf[k]); lo[k] = cf[k] - hi[k]; }
                a[0] = pack2(hi[0], hi[1]); a[1] = pack2(hi[2], hi[3]); a[2] = pack2(lo[0], lo[1]); a[3] = pack2(lo[2], lo[3]);
            }
            *(u32x4*)(smem + L_WFA + p * 16) = a;
            l_rh[p] = rh0; l_rh[CAP + p] = rh1; l_rh[2 * CAP + p] = rh2;
            l_dl[p] = p < nrec ? (u16)loc : (u16)0xffffu;
        }

        for (int ch = 0; ch < nch; ++ch) {
            wait_vm(issued - mA);
            {
                const int lim_ = cur;
                lds_barrier();
                ++epoch;
                if (ch >= 1 && ch + 2 < nch) { DMA_CHUNK(ch + 2, bC); mC = issued; }
                STORE_PENDING(lim_);
            }
            const char* B = smem + (bA - lbase);
            const int eb = ch * 32;
            u32x4 A[4];
#pragma unroll
            for (int blk = 0; blk < 2; ++blk) {
                f32x4 Z[3], Wf[3];
                const u32x2 wa = *(const u32x2*)(smem + L_WFA + (eb + 16 * blk + c16) * 16 + (q & 1) * 8);
#pragma unroll
                for (int gg = 0; gg < 3; ++gg) {
                    Z[gg] = (f32x4){0.f, 0.f, 0.f, 0.f};
                    Wf[gg] = mfma16k16(wa, WR[gg], (f32x4){0.f, 0.f, 0.f, 0.f});
                }
#pragma unroll
                for (int ks = 0; ks < 4; ++ks) {
                    const u32x4 a = *(const u32x4*)(B + (16 * blk + c16) * PITCH + ks * 64 + q * 16);
#pragma unroll
                    for (int gg = 0; gg < 3; ++gg) Z[gg] = mfma16(a, W2[gg][ks], Z[gg]);
                }
                float mvv[4], mvr[4], ms[4];
#pragma unroll
                for (int r = 0; r < 4; ++r) { ms[r] = Z[0][r] * Wf[0][r]; mvv[r] = Z[1][r] * Wf[1][r]; mvr[r] = Z[2][r] * Wf[2][r]; }
                A[0][2 * blk] = pack2(ms[0], ms[1]); A[0][2 * blk + 1] = pack2(ms[2], ms[3]);
#pragma unroll
                for (int x = 0; x < 3; ++x) {
                    const u32x2 vv = tr_read(B + (16 * blk + 4 * q + (c16 >> 2)) * PITCH + 256 * (1 + x) + 32 * w + 8 * (c16 & 3));
                    const f32x4 rh = *(const f32x4*)(l_rh + x * CAP + eb + 16 * blk + 4 * q);
                    const float u0 = mvv[0] * lo16(vv[0]) + mvr[0] * rh[0];
                    const float u1 = mvv[1] * hi16(vv[0]) + mvr[1] * rh[1];
                    const float u2 = mvv[2] * lo16(vv[1]) + mvr[2] * rh[2];
                    const float u3 = mvv[3] * hi16(vv[1]) + mvr[3] * rh[3];
                    A[1 + x][2 * blk] = pack2(u0, u1); A[1 + x][2 * blk + 1] = pack2(u2, u3);
                }
            }
            const u32x2 dA = *(const u32x2*)(l_dl + eb + 4 * q), dB = *(const u32x2*)(l_dl + eb + 16 + 4 * q);
            const int e1 = min(eb + 32, nrec);
            while (true) {
                const int endc = __builtin_amdgcn_readlane((int)rbtab, cur);
                if (endc > eb) {
                    const u32 tg = (u32)(cur * 16 + c16) * 0x10001u;
                    u32x4 S;
                    const u32 dd[4] = {dA[0], dA[1], dB[0], dB[1]};
#pragma unroll
                    for (int i = 0; i < 4; ++i) {
                        u16x2 xx = __builtin_bit_cast(u16x2, dd[i] ^ tg);
                        u16x2 y = __builtin_elementwise_min(xx, (u16x2){1, 1});
                        u16x2 z = y * (u16x2){0xC080, 0xC080} + (u16x2){0x3F80, 0x3F80};
                        S[i] = __builtin_bit_cast(u32, z);
                    }
#pragma unroll
                    for (int x = 0; x < 4; ++x) acc[x] = mfma16(A[x], S, acc[x]);
                }
                if (endc >= e1) break;
                FLUSH_CUR();
            }
            mA = mB; mB = mC;
            const u32 tb = bA; bA = bB; bB = bC; bC = tb;
        }
    }
    if (first) {
        VMWAIT(0);
#pragma unroll
        for (int rb = 0; rb < 7; ++rb) asm volatile("" : "+v"(res[rb][0]), "+v"(res[rb][1]));
    }
    while (cur < 7) FLUSH_CUR();
    EPOCH_BARRIER();
}

extern "C" void kernel_launch(void* const* d_in, const int* in_sizes, int n_in, void* d_out, int out_size, void* d_ws,
                              size_t ws_size, hipStream_t stream) {
    const u16* sv = (const u16*)d_in[0];
    const u16* pos = (const u16*)d_in[1];
    const int* src = (const int*)d_in[2];
    const int* dst = (const int*)d_in[3];
    const u16* freqs = (const u16*)d_in[4];
    const u16* w1 = (const u16*)d_in[5];
    const u16* w2 = (const u16*)d_in[6];
    const u16* wrbf = (const u16*)d_in[7];
    u16* out = (u16*)d_out;
    const int N = in_sizes[0] / 512;
    const int E = in_sizes[2];
    const int NT = (N + TILES - 1) / TILES;
    const int EPB = ((E + G - 1) / G + 3) / 4 * 4;
    const u32 ntmagic = (u32)((0x100000000ull + NT - 1) / NT);
    const int nNodeBlocks = (N + NODES_PB - 1) / NODES_PB;

    char* ws = (char*)d_ws;
    size_t off = 0;
    u16* h = (u16*)(ws + off); off += ((size_t)N * 256 + 255) / 256 * 256;
    u32x4* w2f = (u32x4*)(ws + off); off += 6144 * 16;
    u32x2* wrf = (u32x2*)(ws + off); off += 24 * 64 * 8;
    u32* bins = (u32*)(ws + off);

    const int posBytes = ((N * 6 + 15) / 16) * 16;
    const int preLds = posBytes + TILES * 4 + 64;
    (void)hipFuncSetAttribute((const void*)pre_kernel, hipFuncAttributeMaxDynamicSharedMemorySize, preLds);
    (void)hipFuncSetAttribute((const void*)main_kernel, hipFuncAttributeMaxDynamicSharedMemorySize, MAIN_LDS);
    pre_kernel<<<G + nNodeBlocks + W2_BLOCKS, PRE_T, preLds, stream>>>(sv, pos, src, dst, w1, w2, wrbf, h, w2f, wrf, bins, N, E,
                                                                       EPB, NT, ntmagic, nNodeBlocks);
    main_kernel<<<TILES, 512, MAIN_LDS, stream>>>(sv, pos, src, dst, freqs, h, w2f, wrf, bins, out, N, E, NT);
}
```

```cpp
#include <hip/hip_runtime.h>
#include <stdint.h>

typedef unsigned short u16;
typedef unsigned int u32;
typedef unsigned long long u64;
typedef __attribute__((ext_vector_type(8))) __bf16 bf16x8;
typedef __attribute__((ext_vector_type(2))) __bf16 bf16x2;
typedef __attribute__((ext_vector_type(4))) float f32x4;
typedef __attribute__((ext_vector_type(4))) u32 u32x4;
typedef __attribute__((ext_vector_type(2))) u32 u32x2;
typedef __attribute__((ext_vector_type(4))) int i32x4;
typedef __attribute__((ext_vector_type(4))) short s16x4;
typedef __attribute__((ext_vector_type(2))) u16 u16x2;
typedef __attribute__((address_space(3))) char lds_char;

#define DI __device__ __forceinline__
#define STAMP(k,i)
#define STAMPW(k,i,wv)

constexpr int TILES = 256;
constexpr int G = 128;
constexpr int PRE_T = 1024;
constexpr int NODES_PB = 256;
constexpr int W2_BLOCKS = 6;
constexpr int CAP = 512;
constexpr int RC = 2048;
constexpr int PITCH = 1056;
constexpr int CHUNK_B = 32 * PITCH;
constexpr int SPITCH = 1040;
constexpr int STAGE_B = 16 * SPITCH;

constexpr int L_BUF = 0;
constexpr int L_STG = 3 * CHUNK_B;
constexpr int L_WFA = L_STG + 2 * STAGE_B;
constexpr int L_RH = L_WFA + CAP * 16;
constexpr int L_DL = L_RH + 3 * CAP * 4;
constexpr int L_FIN = L_DL + CAP * 2;
constexpr int L_HIST = L_FIN + RC * 4;
constexpr int L_START = L_HIST + 512;
constexpr int L_POSD = L_START + 544;
constexpr int L_MISC = L_POSD + 1344;
constexpr int MAIN_LDS = L_MISC + 64;
static_assert(MAIN_LDS <= 163840, "lds");

DI float bf2f(u32 b) { return __uint_as_float(b << 16); }
DI float rbf16(float x) { return (float)(__bf16)x; }
DI u32 pack2(float a, float b) { bf16x2 v; v[0] = (__bf16)a; v[1] = (__bf16)b; return __builtin_bit_cast(u32, v); }
DI float lo16(u32 p) { return __uint_as_float(p << 16); }
DI float hi16(u32 p) { return __uint_as_float(p & 0xffff0000u); }

DI u32 wave_incl_scan(u32 v) {
    v += (u32)__builtin_amdgcn_update_dpp(0, (int)v, 0x111, 0xf, 0xf, false);
    v += (u32)__builtin_amdgcn_update_dpp(0, (int)v, 0x112, 0xf, 0xf, false);
    v += (u32)__builtin_amdgcn_update_dpp(0, (int)v, 0x114, 0xf, 0xf, false);
    v += (u32)__builtin_amdgcn_update_dpp(0, (int)v, 0x118, 0xf, 0xf, false);
    v += (u32)__builtin_amdgcn_update_dpp(0, (int)v, 0x142, 0xa, 0xf, false);
    v += (u32)__builtin_amdgcn_update_dpp(0, (int)v, 0x143, 0xc, 0xf, false);
    return v;
}

DI void dma16(const void* gsrc, u32 lds_base) {
    asm volatile("s_mov_b32 m0, %1\n\ts_nop 0\n\tglobal_load_lds_dwordx4 %0, off" ::"v"(gsrc), "s"(lds_base) : "memory");
}
DI void lds_barrier() {
    asm volatile("s_waitcnt lgkmcnt(0)" ::: "memory");
    __builtin_amdgcn_s_barrier();
    asm volatile("" ::: "memory");
}
#define VMWAIT(n) asm volatile("s_waitcnt vmcnt(" #n ")" ::: "memory")
DI void wait_vm(int n) {
    if (n >= 8) {
        if (n >= 12) { if (n >= 16) VMWAIT(16); else if (n >= 14) VMWAIT(14); else VMWAIT(12); }
        else { if (n >= 10) VMWAIT(10); else VMWAIT(8); }
    } else if (n >= 4) {
        if (n >= 6) VMWAIT(6); else VMWAIT(4);
    } else {
        if (n >= 2) VMWAIT(2); else VMWAIT(0);
    }
}
typedef __attribute__((ext_vector_type(4))) short v4i16_t;
DI u32x2 tr_read(const char* p) {
    v4i16_t r = __builtin_amdgcn_ds_read_tr16_b64_v4i16((__attribute__((address_space(3))) v4i16_t*)p);
    return __builtin_bit_cast(u32x2, r);
}
DI f32x4 mfma16(u32x4 a, u32x4 b, f32x4 c) {
    return __builtin_amdgcn_mfma_f32_16x16x32_bf16(__builtin_bit_cast(bf16x8, a), __builtin_bit_cast(bf16x8, b), c, 0, 0, 0);
}
DI f32x4 mfma16k16(u32x2 a, u32x2 b, f32x4 c) {
    return __builtin_amdgcn_mfma_f32_16x16x16bf16_1k(__builtin_bit_cast(s16x4, a), __builtin_bit_cast(s16x4, b), c, 0, 0, 0);
}
DI void aload16(u32x4& d, const void* base, u32 off) {
    asm volatile("global_load_dwordx4 %0, %1, %2" : "=v"(d) : "v"(off), "s"(base) : "memory");
}
DI void aload8(u32x2& d, const void* base, u32 off) {
    asm volatile("global_load_dwordx2 %0, %1, %2" : "=v"(d) : "v"(off), "s"(base) : "memory");
}
DI void aload2(u32& d, const void* base, u32 off) {
    asm volatile("global_load_ushort %0, %1, %2" : "=v"(d) : "v"(off), "s"(base) : "memory");
}

__global__ __launch_bounds__(PRE_T) void pre_kernel(const u16* __restrict__ sv, const u16* __restrict__ pos,
                                                    const int* __restrict__ src, const int* __restrict__ dst,
                                                    const u16* __restrict__ w1, const u16* __restrict__ w2,
                                                    const u16* __restrict__ wrbf, u16* __restrict__ h,
                                                    u32x4* __restrict__ w2f, u32x2* __restrict__ wrf, u32* __restrict__ bins,
                                                    int N, int E, int EPB, int NT, u32 ntmagic, int nNodeBlocks) {
    extern __shared__ __attribute__((aligned(16))) char smem[];
    const int t = threadIdx.x, lane = t & 63;
    const int w = __builtin_amdgcn_readfirstlane(t >> 6);
    const int b = blockIdx.x;
    STAMP(0,0);
    if (b < G) {
        const int g = (b & 7) * (G / 8) + (b >> 3);
        const int posUnits = (N * 6 + 15) >> 4;
        const int posBytes = posUnits * 16;
        u32* hist = (u32*)(smem + posBytes);
        const int e0 = g * EPB + t * 4;
        const int eEnd = min(E, (g + 1) * EPB);
        const int e0c = min(e0, E - 4);
        u32x4 s4, d4;
        aload16(s4, src, (u32)e0c * 4u);
        aload16(d4, dst, (u32)e0c * 4u);
        const u32 lbase = (u32)(size_t)(lds_char*)smem;
        for (int u0 = w * 64; u0 < posUnits; u0 += PRE_T) {
            const int u = u0 + lane;
            if ((u + 1) * 16 <= N * 6) dma16((const char*)pos + (size_t)u * 16, lbase + (u32)u0 * 16u);
        }
        if (t < TILES) hist[t] = 0;
        {
            const int tail0 = (N * 6) & ~15;
            if (t < ((N * 6 - tail0) >> 1)) ((u16*)(smem + tail0))[t] = pos[(tail0 >> 1) + t];
        }
        STAMP(0,1);
        VMWAIT(0);
        STAMP(0,2);
        asm volatile("" : "+v"(s4), "+v"(d4));
        lds_barrier();
        STAMP(0,3);
        const u32* posw = (const u32*)smem;
#pragma unroll
        for (int i = 0; i < 4; ++i) {
            const bool ok = e0c + i >= e0 && e0c + i < eEnd;
            const u32 si = s4[i], di = d4[i];
            const u32 as = (3 * si) >> 1, ad = (3 * di) >> 1;
            const u32 as0 = posw[as], as1 = posw[as + 1];
            const u32 ad0 = posw[ad], ad1 = posw[ad + 1];
            const u32 shs = (si & 1) * 16, shd = (di & 1) * 16;
            const u32 sxy = __builtin_amdgcn_alignbit(as1, as0, shs), dxy = __builtin_amdgcn_alignbit(ad1, ad0, shd);
            const float rx = lo16(sxy) - lo16(dxy);
            const float ry = hi16(sxy) - hi16(dxy);
            const float rz = lo16(as1 >> shs) - lo16(ad1 >> shd);
            const float ss = rx * rx + ry * ry + rz * rz;
            const bool drop = (__float_as_uint(ss) - 0x41CA0000u) <= (0x7E000000u - 0x41CA0000u);
            if (ok && !drop) {
                const u32 tile = __umulhi(di, ntmagic);
                const u32 loc = di - tile * (u32)NT;
                const u32 rank = atomicAdd(&hist[tile], 1u);
                if (rank < 15u) bins[((size_t)tile * G + g) * 16 + 1 + rank] = (loc << 24) | si;
            }
        }
        STAMP(0,4);
        lds_barrier();
        STAMP(0,5);
        if (t < TILES) bins[((size_t)t * G + g) * 16] = hist[t];
        STAMP(0,6);
    } else if (b < G + nNodeBlocks) {
        const int nb = b - G;
        const int n = lane & 15, q = lane >> 4;
        const int node = nb * NODES_PB + w * 16 + n;
        const int nodec = min(node, N - 1);
        u32x4 sb[4];
#pragma unroll
        for (int ks = 0; ks < 4; ++ks) sb[ks] = *(const u32x4*)(sv + (size_t)nodec * 512 + ks * 32 + q * 8);
#pragma unroll
        for (int i = 0; i < 2; ++i) {
            const int u = t + i * PRE_T, k = u >> 4, cu = u & 15;
            u32x4 v = *(const u32x4*)(w1 + (size_t)u * 8);
            if ((k >> 3) & 1) { u32x4 x; x[0] = v[2]; x[1] = v[3]; x[2] = v[0]; x[3] = v[1]; v = x; }
            *(u32x4*)(smem + k * 256 + ((cu * 16) ^ ((k & 3) << 6))) = v;
        }
        STAMP(0,1);
        __syncthreads();
        STAMP(0,2);
        const int r = (lane & 15) >> 2, cc = lane & 3;
#pragma unroll
        for (int tt = 0; tt < 4; ++tt) {
            f32x4 acc[2];
#pragma unroll
            for (int hf = 0; hf < 2; ++hf) {
                acc[hf] = (f32x4){0.f, 0.f, 0.f, 0.f};
#pragma unroll
                for (int ks = 0; ks < 4; ++ks) {
                    const int col = (64 * tt + 16 * cc + 8 * hf) ^ (r << 6) ^ ((q & 1) << 3);
                    const int k0 = 32 * ks + 8 * q + r;
                    const u32x2 a0 = tr_read(smem + k0 * 256 + col);
                    const u32x2 a1 = tr_read(smem + (k0 + 4) * 256 + col);
                    u32x4 a; a[0] = a0[0]; a[1] = a0[1]; a[2] = a1[0]; a[3] = a1[1];
                    acc[hf] = mfma16(a, sb[ks], acc[hf]);
                }
            }
            u32x4 o;
#pragma unroll
            for (int hf = 0; hf < 2; ++hf) {
                float y[4];
#pragma unroll
                for (int i = 0; i < 4; ++i) {
                    const float x = rbf16(acc[hf][i]);
                    y[i] = x * __builtin_amdgcn_rcpf(1.0f + __expf(-x));
                }
                o[2 * hf] = pack2(y[0], y[1]); o[2 * hf + 1] = pack2(y[2], y[3]);
            }
            if (node < N) *(u32x4*)(h + (size_t)node * 128 + 32 * tt + 8 * q) = o;
            STAMP(0,3+tt);
        }
    } else {
        const int T = (b - G - nNodeBlocks) * PRE_T + t;
        const int fi = T >> 6;
        const int ww = fi / 12, gg = (fi % 12) >> 2, ks = fi & 3;
        const int c = lane & 15, q = lane >> 4;
        u32 v[8];
#pragma unroll
        for (int j = 0; j < 8; ++j) v[j] = w2[(size_t)(32 * ks + 8 * q + j) * 384 + 128 * gg + 16 * ww + c];
        u32x4 o;
#pragma unroll
        for (int j = 0; j < 4; ++j) o[j] = v[2 * j] | (v[2 * j + 1] << 16);
        w2f[T] = o;
        if (ks == 0) {
            u32 r[4];
#pragma unroll
            for (int k = 0; k < 4; ++k) r[k] = wrbf[k * 384 + 128 * gg + 16 * ww + c];
            u32x2 p; p[0] = q < 2 ? (r[0] | (r[1] << 16)) : 0u; p[1] = q < 2 ? (r[2] | (r[3] << 16)) : 0u;
            wrf[(ww * 3 + gg) * 64 + lane] = p;
        }
    }
}

#define RING_LOAD0(base, o0, o1)                                                                             \
    asm volatile("global_load_dwordx4 v[240:243], %0, %2\n\tglobal_load_dwordx4 v[244:247], %1, %2" ::"v"(o0), "v"(o1), "s"(base) \
                 : "memory", "v240", "v241", "v242", "v243", "v244", "v245", "v246", "v247")
#define RING_LOAD1(base, o0, o1)                                                                             \
    asm volatile("global_load_dwordx4 v[248:251], %0, %2\n\tglobal_load_dwordx4 v[252:255], %1, %2" ::"v"(o0), "v"(o1), "s"(base) \
                 : "memory", "v248", "v249", "v250", "v251", "v252", "v253", "v254", "v255")
#define RING_WRITE0(la) asm volatile("ds_write_b128 %0, v[240:243]\n\tds_write_b128 %0, v[244:247] offset:1040" ::"v"(la) : "memory")
#define RING_WRITE1(la) asm volatile("ds_write_b128 %0, v[248:251]\n\tds_write_b128 %0, v[252:255] offset:1040" ::"v"(la) : "memory")
DI void dma16s(u32 voff, const void* sbase, u32 lds_base) {
    asm volatile("s_mov_b32 m0, %2\n\ts_nop 0\n\tglobal_load_lds_dwordx4 %0, %1" ::"v"(voff), "s"(sbase), "s"(lds_base) : "memory");
}
DI void astore16nt(u32x4 d, void* base, u32 off) {
    asm volatile("global_store_dwordx4 %0, %1, %2 nt\n\ts_nop 1" ::"v"(off), "v"(d), "s"(base) : "memory");
}
DI u32x4 onehot(u32x2 dA, u32x2 dB, u32 tg, u32 kOne) {
    u32x4 S;
    const u32 kNeg = 0x80008000u;
    asm volatile(
        "v_xor_b32 %0, %4, %8\n\tv_xor_b32 %1, %5, %8\n\tv_xor_b32 %2, %6, %8\n\tv_xor_b32 %3, %7, %8\n\t"
        "v_pk_mad_i16 %0, %0, %9, %10 clamp\n\tv_pk_mad_i16 %1, %1, %9, %10 clamp\n\t"
        "v_pk_mad_i16 %2, %2, %9, %10 clamp\n\tv_pk_mad_i16 %3, %3, %9, %10 clamp\n\ts_nop 1"
        : "=&v"(S[0]), "=&v"(S[1]), "=&v"(S[2]), "=&v"(S[3])
        : "v"(dA[0]), "v"(dA[1]), "v"(dB[0]), "v"(dB[1]), "v"(tg), "s"(kNeg), "v"(kOne));
    return S;
}

__global__ __launch_bounds__(512, 2) __attribute__((amdgpu_num_vgpr(240))) void main_kernel(const u16* __restrict__ sv, const u16* __restrict__ pos,
                                                      const int* __restrict__ src, const int* __restrict__ dst,
                                                      const u16* __restrict__ freqs, const u16* __restrict__ h,
                                                      const u32x4* __restrict__ w2f, const u32x2* __restrict__ wrf,
                                                      const u32* __restrict__ bins, u16* __restrict__ out, int N, int E,
                                                      int NT) {
    extern __shared__ __attribute__((aligned(16))) char smem[];
    const int t = threadIdx.x, lane = t & 63;
    const int w = __builtin_amdgcn_readfirstlane(t >> 6);
    const int b = blockIdx.x;
    const int tile = (b & 7) * (TILES / 8) + (b >> 3);
    const int c16 = lane & 15, q = lane >> 4;
    const int node0 = tile * NT;
    const int nvalid = min(NT, N - node0);
    if (nvalid <= 0) return;
    const u32 lbase = (u32)(size_t)(lds_char*)smem;
    STAMP(1,0);

    u32x4 bw;
    aload16(bw, (const char*)bins + (size_t)tile * (G * 64), (u32)t * 16u);
    u32 pdv;
    aload2(pdv, pos + (size_t)node0 * 3, (u32)min(t, 3 * nvalid - 1) * 2u);
    u32x4 W2[3][4];
#pragma unroll
    for (int gg = 0; gg < 3; ++gg)
#pragma unroll
        for (int ks = 0; ks < 4; ++ks) aload16(W2[gg][ks], w2f, (u32)((w * 12 + gg * 4 + ks) * 64 + lane) * 16u);
    u32x2 WR[3];
#pragma unroll
    for (int gg = 0; gg < 3; ++gg) aload8(WR[gg], wrf, (u32)((w * 3 + gg) * 64 + lane) * 8u);
    const char* const rbase = (const char*)sv + (size_t)node0 * 1024;
#define RES_OFF(rb, i) ((u32)min((rb) * 16 + 2 * w + (i), nvalid - 1) * 1024u + (u32)lane * 16u)
    u32x4 T0[2], T1[2];
    aload16(T0[0], rbase, RES_OFF(0, 0)); aload16(T0[1], rbase, RES_OFF(0, 1));
    aload16(T1[0], rbase, RES_OFF(1, 0)); aload16(T1[1], rbase, RES_OFF(1, 1));
    const float fr0 = bf2f(freqs[0]), fr1 = bf2f(freqs[1]), fr2 = bf2f(freqs[2]), fr3 = bf2f(freqs[3]);

    u32* const l_tmp = (u32*)(smem + L_STG);
    u32* const l_fin = (u32*)(smem + L_FIN);
    u32* const l_hist = (u32*)(smem + L_HIST);
    u32* const l_start = (u32*)(smem + L_START);
    float* const l_posd = (float*)(smem + L_POSD);
    u32* const l_misc = (u32*)(smem + L_MISC);
    u16* const l_dl = (u16*)(smem + L_DL);
    float* const l_rh = (float*)(smem + L_RH);

    if (t < 128) l_hist[t] = 0;
    if (t == 128) l_misc[0] = 0;
    lds_barrier();

    VMWAIT(19);
    asm volatile("" : "+v"(bw), "+v"(pdv));
    STAMP(1,1);
    if (t < 3 * nvalid) l_posd[t] = bf2f(pdv);
    const u32 cnt = (u32)__builtin_amdgcn_update_dpp(0, (int)bw[0], 0x00, 0xf, 0xf, false);
    if (cnt > 15u) l_misc[0] = 1;
    u32 rk[4];
    bool vl[4];
#pragma unroll
    for (int i = 0; i < 4; ++i) {
        const u32 widx = 4 * (t & 3) + i;
        vl[i] = widx >= 1 && widx <= cnt && cnt <= 15u;
        rk[i] = 0;
        if (vl[i]) rk[i] = atomicAdd(&l_hist[bw[i] >> 24], 1u);
    }
    lds_barrier();
    u32 starts;
    int total;
    {
        const u32x2 hh = *(const u32x2*)(l_hist + 2 * lane);
        const u32 inc = wave_incl_scan(hh[0] + hh[1]);
        const u32 s0 = inc - hh[0] - hh[1], s1 = s0 + hh[0];
        starts = s0 | (s1 << 16);
        total = __builtin_amdgcn_readlane((int)inc, 63);
        if (w == 0) { u32x2 sx; sx[0] = s0; sx[1] = s1; *(u32x2*)(l_start + 2 * lane) = sx; if (lane == 63) l_start[128] = inc; }
    }
    const bool slow = __builtin_amdgcn_readfirstlane((int)l_misc[0]) != 0;
#pragma unroll
    for (int i = 0; i < 4; ++i) {
        const u32 loc = (bw[i] >> 24) & 127u;
        const u32 p = (u32)__builtin_amdgcn_ds_bpermute((int)((loc >> 1) << 2), (int)starts);
        const u32 st = (loc & 1) ? (p >> 16) : (p & 0xffffu);
        if (vl[i]) l_tmp[st + rk[i]] = bw[i];
    }
    if (t >= 480) l_fin[total + t - 480] = 0;
    lds_barrier();
    for (int p = t; p < total; p += 512) {
        const u32 rec = l_tmp[p];
        const u32 loc = rec >> 24;
        const int s = (int)l_start[loc], e = (int)l_start[loc + 1];
        int rank = 0;
        for (int j = s; j < e; j += 2) {
            const u32 k0 = l_tmp[j], k1 = l_tmp[j + 1];
            rank += (k0 < rec || (k0 == rec && j < p)) ? 1 : 0;
            rank += (j + 1 < e && (k1 < rec || (k1 == rec && j + 1 < p))) ? 1 : 0;
        }
        l_fin[s + rank] = rec;
    }
    lds_barrier();
    STAMP(1,2);

    f32x4 acc[4];
#pragma unroll
    for (int x = 0; x < 4; ++x) acc[x] = (f32x4){0.f, 0.f, 0.f, 0.f};
    int cur = 0;
    int sto = 0;
    int epoch = 0, ePrev1 = -1, ePrev2 = -1;
    int issued = 0;
    int mR0 = 0, mR1 = 0;

    const char* const gb = (const char*)h < (const char*)sv ? (const char*)h : (const char*)sv;
    const size_t dh = (size_t)((const char*)h - gb), ds = (size_t)((const char*)sv - gb);
    const bool nearp = (dh > ds ? dh : ds) + (size_t)N * 1024 < 0xFFFFFFFFull;
    const u32 dlane = (u32)(lane < 16 ? dh : ds) + (u32)lane * 16u;
    const u32 dshift = lane < 16 ? 8u : 10u;
    const char* const dbase = (lane < 16 ? (const char*)h : (const char*)sv) + lane * 16;
    char* const obase = (char*)out + (size_t)node0 * 1024;
    const u32 kOne = 0x3F803F80u;
    u32x2 ident;
    ident[0] = (4 * q == c16 ? 0x3F80u : 0u) | (4 * q + 1 == c16 ? 0x3F800000u : 0u);
    ident[1] = (4 * q + 2 == c16 ? 0x3F80u : 0u) | (4 * q + 3 == c16 ? 0x3F800000u : 0u);
    u32 tg = (u32)(2 * c16) * 0x10001u;

#define STORE_PENDING(lim)                                                                                  \
    while (sto < (lim)) {                                                                                   \
        char* sp_ = smem + L_STG + (sto & 1) * STAGE_B + 2 * w * SPITCH + lane * 16;                        \
        const u32x4 d0_ = *(const u32x4*)sp_, d1_ = *(const u32x4*)(sp_ + SPITCH);                          \
        const u32 la_ = (u32)(size_t)(lds_char*)sp_;                                                        \
        if (sto < 5) {                                                                                      \
            if (sto & 1) {                                                                                  \
                wait_vm(issued - mR1);                                                                      \
                RING_WRITE1(la_);                                                                           \
                if (sto < 3) {                                                                              \
                    RING_LOAD1(rbase, RES_OFF(sto + 4, 0), RES_OFF(sto + 4, 1));                            \
                    issued += 2; mR1 = issued;                                                              \
                }                                                                                           \
            } else {                                                                                        \
                wait_vm(issued - mR0);                                                                      \
                RING_WRITE0(la_);                                                                           \
                if (sto < 3) {                                                                              \
                    RING_LOAD0(rbase, RES_OFF(sto + 4, 0), RES_OFF(sto + 4, 1));                            \
                    issued += 2; mR0 = issued;                                                              \
                }                                                                                           \
            }                                                                                               \
        }                                                                                                   \
        const int row_ = sto * 16 + 2 * w;                                                                  \
        const u32 oo_ = (u32)row_ * 1024u + (u32)lane * 16u;                                                \
        if (row_ < nvalid) { astore16nt(d0_, obase, oo_); ++issued; }                                       \
        if (row_ + 1 < nvalid) { astore16nt(d1_, obase, oo_ + 1024u); ++issued; }                           \
        ++sto;                                                                                              \
    }
#define EPOCH_BARRIER()                                                                                     \
    do {                                                                                                    \
        const int lim_ = cur;                                                                               \
        lds_barrier();                                                                                      \
        ++epoch;                                                                                            \
        STORE_PENDING(lim_);                                                                                \
    } while (0)
#define FLUSH_CUR()                                                                                         \
    do {                                                                                                    \
        while (epoch < ePrev2 + 2) EPOCH_BARRIER();                                                         \
        char* sb_ = smem + L_STG + (cur & 1) * STAGE_B + 32 * w;                                            \
        u32x2 rt_[4];                                                                                       \
        _Pragma("unroll") for (int x_ = 0; x_ < 4; ++x_)                                                    \
            rt_[x_] = tr_read(sb_ + (4 * q + (c16 >> 2)) * SPITCH + 8 * (c16 & 3) + 256 * x_);              \
        _Pragma("unroll") for (int x_ = 0; x_ < 4; ++x_) {                                                  \
            const f32x4 f_ = mfma16k16(rt_[x_], ident, acc[x_]);                                            \
            u32x2 o_; o_[0] = pack2(f_[0], f_[1]); o_[1] = pack2(f_[2], f_[3]);                             \
            *(u32x2*)(sb_ + c16 * SPITCH + 8 * q + 256 * x_) = o_;                                          \
        }                                                                                                   \
        ePrev2 = ePrev1; ePrev1 = epoch;                                                                    \
        ++cur; tg += 32u * 0x10001u;                                                                        \
    } while (0)
#define SEG()                                                                                               \
    do {                                                                                                    \
        int endc_ = __builtin_amdgcn_readlane((int)rbtab, cur);                                             \
        if (endc_ > sEb) {                                                                                  \
            const u32x4 S_ = onehot(dA, dB, tg, kOne);                                                      \
            _Pragma("unroll") for (int x_ = 0; x_ < 4; ++x_) acc[x_] = mfma16(A[x_], S_, acc[x_]);          \
        }                                                                                                   \
        while (endc_ < sE1) {                                                                               \
            FLUSH_CUR();                                                                                    \
            endc_ = __builtin_amdgcn_readlane((int)rbtab, cur);                                             \
            const u32x4 S_ = onehot(dA, dB, tg, kOne);                                                      \
            _Pragma("unroll") for (int x_ = 0; x_ < 4; ++x_)                                                \
                acc[x_] = mfma16(A[x_], S_, (f32x4){0.f, 0.f, 0.f, 0.f});                                   \
        }                                                                                                   \
    } while (0)

    int W0 = 0;
    int slowR = 0, slowE = 0;
    bool first = true;
    for (;;) {
        int nrec;
        u32 rbtab;
        const u32* list;
        if (!slow) {
            if (W0 >= total) break;
            nrec = min(CAP, total - W0);
            list = l_fin + W0;
            const u32 nx = (u32)__builtin_amdgcn_ds_bpermute(((lane & 7) + 1) * 32, (int)starts) & 0xffffu;
            rbtab = (u32)min(max((int)nx - W0, 0), nrec);
            W0 += CAP;
            if (!first) lds_barrier();
        } else {
            int n = 0;
            lds_barrier();
            while (slowR < 7) {
                if (slowE >= E) {
                    ++slowR; slowE = 0;
                    if (n > 0) { --slowR; slowE = E; break; }
                    continue;
                }
                const int e = slowE + t;
                int di = -1, si = 0;
                if (e < E) { di = dst[e]; si = src[e]; }
                asm volatile("" : "+v"(di), "+v"(si));
                const int loc = di - node0;
                const bool m = e < E && loc >= 0 && loc < nvalid && (loc >> 4) == slowR;
                const u64 bal = __ballot(m);
                const int pw = __builtin_amdgcn_mbcnt_hi((u32)(bal >> 32), __builtin_amdgcn_mbcnt_lo((u32)bal, 0));
                if (lane == 0) l_hist[w] = (u32)__popcll(bal);
                lds_barrier();
                int basep = 0, all = 0;
#pragma unroll
                for (int i = 0; i < 8; ++i) { const int c = (int)l_hist[i]; basep += i < w ? c : 0; all += c; }
                all = __builtin_amdgcn_readfirstlane(all);
                const bool fits = n + all <= CAP;
                if (fits && m) l_fin[n + basep + pw] = ((u32)loc << 24) | (u32)si;
                lds_barrier();
                if (!fits) break;
                n += all; slowE += 512;
            }
            if (n == 0) {
                if (slowR >= 7) break;
            }
            if (t < 32) l_fin[n + t] = 0;
            lds_barrier();
            nrec = n;
            list = l_fin;
            rbtab = lane < slowR ? 0u : (u32)n;
            if (slowE >= E) { ++slowR; slowE = 0; }
            if (n == 0) continue;
        }
        const int nch = (nrec + 31) >> 5;
        const u32 rec = list[min(t, nrec - 1)];
        const u32 rsrc = rec & 0xffffffu;
        u32 px, py, pz;
        aload2(px, pos, rsrc * 6u);
        aload2(py, pos + 1, rsrc * 6u);
        aload2(pz, pos + 2, rsrc * 6u);
        issued += 3;
        const int mPos = issued;
        int mA, mB, mC;
        u32 bA = lbase + L_BUF, bB = lbase + L_BUF + CHUNK_B, bC = lbase + L_BUF + 2 * CHUNK_B;
        const u32* const listw = list + 4 * w;
#define DMA_CHUNK(r4, buf)                                                                                  \
    do {                                                                                                    \
        if (nearp) {                                                                                        \
            _Pragma("unroll") for (int i_ = 0; i_ < 4; ++i_)                                                \
                dma16s(((r4)[i_] << dshift) + dlane, gb, (buf) + (u32)(4 * w + i_) * PITCH);                \
        } else {                                                                                            \
            _Pragma("unroll") for (int i_ = 0; i_ < 4; ++i_)                                                \
                dma16(dbase + ((r4)[i_] << dshift), (buf) + (u32)(4 * w + i_) * PITCH);                     \
        }                                                                                                   \
        issued += 4;                                                                                        \
    } while (0)
        u32x4 nr4;
        {
            const u32x4 r0 = *(const u32x4*)listw, r1 = *(const u32x4*)(listw + 32), r2 = *(const u32x4*)(listw + 64);
            nr4 = *(const u32x4*)(listw + 96);
            DMA_CHUNK(r0, bA);
            mA = issued;
            if (nch > 1) DMA_CHUNK(r1, bB);
            mB = issued;
            if (nch > 2) DMA_CHUNK(r2, bC);
            mC = issued;
        }
        if (first) {
            RING_LOAD0(rbase, RES_OFF(2, 0), RES_OFF(2, 1));
            issued += 2; mR0 = issued;
            RING_LOAD1(rbase, RES_OFF(3, 0), RES_OFF(3, 1));
            issued += 2; mR1 = issued;
        }
        STAMP(1,3);
        wait_vm(issued - mPos);
        asm volatile("" : "+v"(px), "+v"(py), "+v"(pz));
        STAMP(1,4);
        if (first) {
#pragma unroll
            for (int gg = 0; gg < 3; ++gg) {
                asm volatile("" : "+v"(WR[gg]));
#pragma unroll
                for (int ks = 0; ks < 4; ++ks) asm volatile("" : "+v"(W2[gg][ks]));
            }
            asm volatile("" : "+v"(T0[0]), "+v"(T0[1]), "+v"(T1[0]), "+v"(T1[1]));
            char* sp = smem + L_STG + 2 * w * SPITCH + lane * 16;
            *(u32x4*)sp = T0[0]; *(u32x4*)(sp + SPITCH) = T0[1];
            *(u32x4*)(sp + STAGE_B) = T1[0]; *(u32x4*)(sp + STAGE_B + SPITCH) = T1[1];
            first = false;
        }
        if (t < nch * 32) {
            u32x4 a = (u32x4){0u, 0u, 0u, 0u};
            float rh0 = 0.f, rh1 = 0.f, rh2 = 0.f;
            const u32 loc = rec >> 24;
            if (t < nrec) {
                const float rx = rbf16(bf2f(px) - l_posd[3 * loc]);
                const float ry = rbf16(bf2f(py) - l_posd[3 * loc + 1]);
                const float rz = rbf16(bf2f(pz) - l_posd[3 * loc + 2]);
                const float ss = rbf16(rbf16(rx * rx) + rbf16(ry * ry) + rbf16(rz * rz));
                const float d = rbf16(sqrtf(rbf16(ss + 1e-8f)));
                const float inv = __builtin_amdgcn_rcpf(d);
                rh0 = rbf16(rx * inv); rh1 = rbf16(ry * inv); rh2 = rbf16(rz * inv);
                const float i2pi = 0.15915494309189535f;
                float ang = rbf16(rbf16(3.14159265358979323846f * d) / 5.0f);
                float fc = rbf16(__builtin_amdgcn_cosf(__builtin_amdgcn_fractf(ang * i2pi)));
                fc = rbf16(0.5f * rbf16(fc + 1.0f));
                fc = d < 5.0f ? fc : 0.0f * d;
                float cf[4];
                const float fr[4] = {fr0, fr1, fr2, fr3};
#pragma unroll
                for (int k = 0; k < 4; ++k) {
                    const float arg = rbf16(d * fr[k]);
                    const float sn = rbf16(__builtin_amdgcn_sinf(__builtin_amdgcn_fractf(arg * i2pi)));
                    cf[k] = rbf16(sn * inv) * fc;
                }
                float hi[4], lo[4];
#pragma unroll
                for (int k = 0; k < 4; ++k) { hi[k] = rbf16(cf[k]); lo[k] = cf[k] - hi[k]; }
                a[0] = pack2(hi[0], hi[1]); a[1] = pack2(hi[2], hi[3]); a[2] = pack2(lo[0], lo[1]); a[3] = pack2(lo[2], lo[3]);
            }
            *(u32x4*)(smem + L_WFA + t * 16) = a;
            l_rh[t] = rh0; l_rh[CAP + t] = rh1; l_rh[2 * CAP + t] = rh2;
            l_dl[t] = t < nrec ? (u16)(2 * loc) : (u16)0x7FFEu;
        }
        STAMP(1,5);

        u32x4 A[4];
        u32x2 dA, dB;
        int sEb = 0, sE1 = 0;
#pragma unroll
        for (int x = 0; x < 4; ++x) A[x] = (u32x4){0u, 0u, 0u, 0u};
        dA = dB = (u32x2){0u, 0u};
        for (int ch = 0; ch < nch; ++ch) {
            wait_vm(issued - mA);
            const int lim_ = cur;
            lds_barrier();
            ++epoch;
            if (ch < 6) STAMP(1,6+3*ch);
            const char* B = smem + (bA - lbase);
            const int eb = ch * 32;
            u32x2 wa[2];
            u32x4 a0[4];
#pragma unroll
            for (int blk = 0; blk < 2; ++blk) wa[blk] = *(const u32x2*)(smem + L_WFA + (eb + 16 * blk + c16) * 16 + (q & 1) * 8);
#pragma unroll
            for (int ks = 0; ks < 4; ++ks) a0[ks] = *(const u32x4*)(B + c16 * PITCH + ks * 64 + q * 16);
            if (ch >= 1 && ch + 2 < nch) { DMA_CHUNK(nr4, bC); mC = issued; }
            if (ch >= 1) nr4 = *(const u32x4*)(listw + (ch + 3) * 32);
            STORE_PENDING(lim_);
            if (ch < 6) STAMP(1,7+3*ch);
            if (ch >= 1) SEG();
            if (ch < 6) STAMP(1,8+3*ch);
#pragma unroll
            for (int blk = 0; blk < 2; ++blk) {
                f32x4 Z[3], Wf[3];
#pragma unroll
                for (int gg = 0; gg < 3; ++gg) {
                    Z[gg] = (f32x4){0.f, 0.f, 0.f, 0.f};
                    Wf[gg] = mfma16k16(wa[blk], WR[gg], (f32x4){0.f, 0.f, 0.f, 0.f});
                }
#pragma unroll
                for (int ks = 0; ks < 4; ++ks) {
                    const u32x4 a = blk == 0 ? a0[ks] : *(const u32x4*)(B + (16 + c16) * PITCH + ks * 64 + q * 16);
#pragma unroll
                    for (int gg = 0; gg < 3; ++gg) Z[gg] = mfma16(a, W2[gg][ks], Z[gg]);
                }
                const f32x4 ms = Z[0] * Wf[0], mvv = Z[1] * Wf[1], mvr = Z[2] * Wf[2];
                A[0][2 * blk] = pack2(ms[0], ms[1]); A[0][2 * blk + 1] = pack2(ms[2], ms[3]);
#pragma unroll
                for (int x = 0; x < 3; ++x) {
                    const u32x2 vv = tr_read(B + (16 * blk + 4 * q + (c16 >> 2)) * PITCH + 256 * (1 + x) + 32 * w + 8 * (c16 & 3));
                    const f32x4 rh = *(const f32x4*)(l_rh + x * CAP + eb + 16 * blk + 4 * q);
                    const f32x4 vf = mfma16k16(ident, vv, (f32x4){0.f, 0.f, 0.f, 0.f});
                    const f32x4 u = mvv * vf + mvr * rh;
                    A[1 + x][2 * blk] = pack2(u[0], u[1]); A[1 + x][2 * blk + 1] = pack2(u[2], u[3]);
                }
            }
            dA = *(const u32x2*)(l_dl + eb + 4 * q); dB = *(const u32x2*)(l_dl + eb + 16 + 4 * q);
            sEb = eb; sE1 = min(eb + 32, nrec);
            mA = mB; mB = mC;
            const u32 tb = bA; bA = bB; bB = bC; bC = tb;
        }
        SEG();
    }
    STAMP(1,24);
    if (first) {
        RING_LOAD0(rbase, RES_OFF(2, 0), RES_OFF(2, 1));
        issued += 2; mR0 = issued;
        RING_LOAD1(rbase, RES_OFF(3, 0), RES_OFF(3, 1));
        issued += 2; mR1 = issued;
        VMWAIT(4);
        asm volatile("" : "+v"(T0[0]), "+v"(T0[1]), "+v"(T1[0]), "+v"(T1[1]));
        char* sp = smem + L_STG + 2 * w * SPITCH + lane * 16;
        *(u32x4*)sp = T0[0]; *(u32x4*)(sp + SPITCH) = T0[1];
        *(u32x4*)(sp + STAGE_B) = T1[0]; *(u32x4*)(sp + STAGE_B + SPITCH) = T1[1];
        lds_barrier();
        ++epoch;
    }
    while (cur < 7) {
        FLUSH_CUR();
#pragma unroll
        for (int x = 0; x < 4; ++x) acc[x] = (f32x4){0.f, 0.f, 0.f, 0.f};
    }
    EPOCH_BARRIER();
    STAMP(1,25);
}

extern "C" void kernel_launch(void* const* d_in, const int* in_sizes, int n_in, void* d_out, int out_size, void* d_ws,
                              size_t ws_size, hipStream_t stream) {
    const u16* sv = (const u16*)d_in[0];
    const u16* pos = (const u16*)d_in[1];
    const int* src = (const int*)d_in[2];
    const int* dst = (const int*)d_in[3];
    const u16* freqs = (const u16*)d_in[4];
    const u16* w1 = (const u16*)d_in[5];
    const u16* w2 = (const u16*)d_in[6];
    const u16* wrbf = (const u16*)d_in[7];
    u16* out = (u16*)d_out;
    const int N = in_sizes[0] / 512;
    const int E = in_sizes[2];
    const int NT = (N + TILES - 1) / TILES;
    const int EPB = ((E + G - 1) / G + 3) / 4 * 4;
    const u32 ntmagic = (u32)((0x100000000ull + NT - 1) / NT);
    const int nNodeBlocks = (N + NODES_PB - 1) / NODES_PB;

    char* ws = (char*)d_ws;
    size_t off = 0;
    u16* h = (u16*)(ws + off); off += ((size_t)N * 256 + 255) / 256 * 256;
    u32x4* w2f = (u32x4*)(ws + off); off += 6144 * 16;
    u32x2* wrf = (u32x2*)(ws + off); off += 24 * 64 * 8;
    u32* bins = (u32*)(ws + off);

    const int posBytes = ((N * 6 + 15) / 16) * 16;
    const int preLds = posBytes + TILES * 4 + 64;
    (void)hipFuncSetAttribute((const void*)pre_kernel, hipFuncAttributeMaxDynamicSharedMemorySize, preLds);
    (void)hipFuncSetAttribute((const void*)main_kernel, hipFuncAttributeMaxDynamicSharedMemorySize, MAIN_LDS);
    pre_kernel<<<G + nNodeBlocks + W2_BLOCKS, PRE_T, preLds, stream>>>(sv, pos, src, dst, w1, w2, wrbf, h, w2f, wrf, bins, N, E,
                                                                       EPB, NT, ntmagic, nNodeBlocks);
    main_kernel<<<TILES, 512, MAIN_LDS, stream>>>(sv, pos, src, dst, freqs, h, w2f, wrf, bins, out, N, E, NT);
}
```

```cpp
#include <hip/hip_runtime.h>
#include <stdint.h>

typedef unsigned short u16;
typedef unsigned int u32;
typedef unsigned long long u64;
typedef __attribute__((ext_vector_type(8))) __bf16 bf16x8;
typedef __attribute__((ext_vector_type(2))) __bf16 bf16x2;
typedef __attribute__((ext_vector_type(4))) float f32x4;
typedef __attribute__((ext_vector_type(4))) u32 u32x4;
typedef __attribute__((ext_vector_type(2))) u32 u32x2;
typedef __attribute__((ext_vector_type(4))) int i32x4;
typedef __attribute__((ext_vector_type(4))) short s16x4;
typedef __attribute__((ext_vector_type(2))) u16 u16x2;
typedef __attribute__((address_space(3))) char lds_char;

#define DI __device__ __forceinline__
#define STAMP(k,i)
#define STAMPW(k,i,wv)
#define MST(i)
#define MST_DECL
#define MST_OUT()

constexpr int TILES = 256;
constexpr int G = 128;
constexpr int PRE_T = 1024;
constexpr int NODES_PB = 256;
constexpr int W2_BLOCKS = 6;
constexpr int CAP = 512;
constexpr int RC = 2048;
constexpr int PITCH = 1056;
constexpr int CHUNK_B = 32 * PITCH;
constexpr int SPITCH = 1040;
constexpr int STAGE_B = 16 * SPITCH;

constexpr int L_BUF = 0;
constexpr int L_STG = 3 * CHUNK_B;
constexpr int L_WFA = L_STG + 2 * STAGE_B;
constexpr int L_RH = L_WFA + CAP * 16;
constexpr int L_DL = L_RH + 3 * CAP * 4;
constexpr int L_FIN = L_DL + CAP * 2;
constexpr int L_HIST = L_FIN + RC * 4;
constexpr int L_START = L_HIST + 512;
constexpr int L_POSD = L_START + 544;
constexpr int L_MISC = L_POSD + 1344;
constexpr int MAIN_LDS = L_MISC + 64;
static_assert(MAIN_LDS <= 163840, "lds");

DI float bf2f(u32 b) { return __uint_as_float(b << 16); }
DI float rbf16(float x) { return (float)(__bf16)x; }
DI u32 pack2(float a, float b) { bf16x2 v; v[0] = (__bf16)a; v[1] = (__bf16)b; return __builtin_bit_cast(u32, v); }
DI float lo16(u32 p) { return __uint_as_float(p << 16); }
DI float hi16(u32 p) { return __uint_as_float(p & 0xffff0000u); }

DI u32 wave_incl_scan(u32 v) {
    v += (u32)__builtin_amdgcn_update_dpp(0, (int)v, 0x111, 0xf, 0xf, false);
    v += (u32)__builtin_amdgcn_update_dpp(0, (int)v, 0x112, 0xf, 0xf, false);
    v += (u32)__builtin_amdgcn_update_dpp(0, (int)v, 0x114, 0xf, 0xf, false);
    v += (u32)__builtin_amdgcn_update_dpp(0, (int)v, 0x118, 0xf, 0xf, false);
    v += (u32)__builtin_amdgcn_update_dpp(0, (int)v, 0x142, 0xa, 0xf, false);
    v += (u32)__builtin_amdgcn_update_dpp(0, (int)v, 0x143, 0xc, 0xf, false);
    return v;
}

DI void dma16(const void* gsrc, u32 lds_base) {
    asm volatile("s_mov_b32 m0, %1\n\ts_nop 0\n\tglobal_load_lds_dwordx4 %0, off" ::"v"(gsrc), "s"(lds_base) : "memory");
}
DI void lds_barrier() {
    asm volatile("s_waitcnt lgkmcnt(0)" ::: "memory");
    __builtin_amdgcn_s_barrier();
    asm volatile("" ::: "memory");
}
#define VMWAIT(n) asm volatile("s_waitcnt vmcnt(" #n ")" ::: "memory")
DI void wait_vm(int n) {
    if (n >= 8) {
        if (n >= 12) { if (n >= 16) VMWAIT(16); else if (n >= 14) VMWAIT(14); else VMWAIT(12); }
        else { if (n >= 10) VMWAIT(10); else VMWAIT(8); }
    } else if (n >= 4) {
        if (n >= 6) VMWAIT(6); else VMWAIT(4);
    } else {
        if (n >= 2) VMWAIT(2); else VMWAIT(0);
    }
}
typedef __attribute__((ext_vector_type(4))) short v4i16_t;
DI u32x2 tr_read(const char* p) {
    v4i16_t r = __builtin_amdgcn_ds_read_tr16_b64_v4i16((__attribute__((address_space(3))) v4i16_t*)p);
    return __builtin_bit_cast(u32x2, r);
}
DI f32x4 mfma16(u32x4 a, u32x4 b, f32x4 c) {
    return __builtin_amdgcn_mfma_f32_16x16x32_bf16(__builtin_bit_cast(bf16x8, a), __builtin_bit_cast(bf16x8, b), c, 0, 0, 0);
}
DI f32x4 mfma16k16(u32x2 a, u32x2 b, f32x4 c) {
    return __builtin_amdgcn_mfma_f32_16x16x16bf16_1k(__builtin_bit_cast(s16x4, a), __builtin_bit_cast(s16x4, b), c, 0, 0, 0);
}
DI void aload16(u32x4& d, const void* base, u32 off) {
    asm volatile("global_load_dwordx4 %0, %1, %2" : "=v"(d) : "v"(off), "s"(base) : "memory");
}
DI void aload8(u32x2& d, const void* base, u32 off) {
    asm volatile("global_load_dwordx2 %0, %1, %2" : "=v"(d) : "v"(off), "s"(base) : "memory");
}
DI void aload2(u32& d, const void* base, u32 off) {
    asm volatile("global_load_ushort %0, %1, %2" : "=v"(d) : "v"(off), "s"(base) : "memory");
}

__global__ __launch_bounds__(PRE_T) void pre_kernel(const u16* __restrict__ sv, const u16* __restrict__ pos,
                                                    const int* __restrict__ src, const int* __restrict__ dst,
                                                    const u16* __restrict__ w1, const u16* __restrict__ w2,
                                                    const u16* __restrict__ wrbf, u16* __restrict__ h,
                                                    u32x4* __restrict__ w2f, u32x2* __restrict__ wrf, u32* __restrict__ bins,
                                                    int N, int E, int EPB, int NT, u32 ntmagic, int nNodeBlocks) {
    extern __shared__ __attribute__((aligned(16))) char smem[];
    const int t = threadIdx.x, lane = t & 63;
    const int w = __builtin_amdgcn_readfirstlane(t >> 6);
    const int b = blockIdx.x;
    STAMP(0,0);
    if (b < G) {
        const int g = (b & 7) * (G / 8) + (b >> 3);
        const int posUnits = (N * 6 + 15) >> 4;
        const int posBytes = posUnits * 16;
        u32* hist = (u32*)(smem + posBytes);
        const int e0 = g * EPB + t * 4;
        const int eEnd = min(E, (g + 1) * EPB);
        const int e0c = min(e0, E - 4);
        u32x4 s4, d4;
        aload16(s4, src, (u32)e0c * 4u);
        aload16(d4, dst, (u32)e0c * 4u);
        const u32 lbase = (u32)(size_t)(lds_char*)smem;
        for (int u0 = w * 64; u0 < posUnits; u0 += PRE_T) {
            const int u = u0 + lane;
            if ((u + 1) * 16 <= N * 6) dma16((const char*)pos + (size_t)u * 16, lbase + (u32)u0 * 16u);
        }
        if (t < TILES) hist[t] = 0;
        {
            const int tail0 = (N * 6) & ~15;
            if (t < ((N * 6 - tail0) >> 1)) ((u16*)(smem + tail0))[t] = pos[(tail0 >> 1) + t];
        }
        STAMP(0,1);
        VMWAIT(0);
        STAMP(0,2);
        asm volatile("" : "+v"(s4), "+v"(d4));
        lds_barrier();
        STAMP(0,3);
        const u32* posw = (const u32*)smem;
#pragma unroll
        for (int i = 0; i < 4; ++i) {
            const bool ok = e0c + i >= e0 && e0c + i < eEnd;
            const u32 si = s4[i], di = d4[i];
            const u32 as = (3 * si) >> 1, ad = (3 * di) >> 1;
            const u32 as0 = posw[as], as1 = posw[as + 1];
            const u32 ad0 = posw[ad], ad1 = posw[ad + 1];
            const u32 shs = (si & 1) * 16, shd = (di & 1) * 16;
            const u32 sxy = __builtin_amdgcn_alignbit(as1, as0, shs), dxy = __builtin_amdgcn_alignbit(ad1, ad0, shd);
            const float rx = lo16(sxy) - lo16(dxy);
            const float ry = hi16(sxy) - hi16(dxy);
            const float rz = lo16(as1 >> shs) - lo16(ad1 >> shd);
            const float ss = rx * rx + ry * ry + rz * rz;
            const bool drop = (__float_as_uint(ss) - 0x41CA0000u) <= (0x7E000000u - 0x41CA0000u);
            if (ok && !drop) {
                const u32 tile = __umulhi(di, ntmagic);
                const u32 loc = di - tile * (u32)NT;
                const u32 rank = atomicAdd(&hist[tile], 1u);
                if (rank < 15u) bins[((size_t)tile * G + g) * 16 + 1 + rank] = (loc << 24) | si;
            }
        }
        STAMP(0,4);
        lds_barrier();
        STAMP(0,5);
        if (t < TILES) bins[((size_t)t * G + g) * 16] = hist[t];
        STAMP(0,6);
    } else if (b < G + nNodeBlocks) {
        const int nb = b - G;
        const int n = lane & 15, q = lane >> 4;
        const int node = nb * NODES_PB + w * 16 + n;
        const int nodec = min(node, N - 1);
        u32x4 sb[4];
#pragma unroll
        for (int ks = 0; ks < 4; ++ks) sb[ks] = *(const u32x4*)(sv + (size_t)nodec * 512 + ks * 32 + q * 8);
#pragma unroll
        for (int i = 0; i < 2; ++i) {
            const int u = t + i * PRE_T, k = u >> 4, cu = u & 15;
            u32x4 v = *(const u32x4*)(w1 + (size_t)u * 8);
            if ((k >> 3) & 1) { u32x4 x; x[0] = v[2]; x[1] = v[3]; x[2] = v[0]; x[3] = v[1]; v = x; }
            *(u32x4*)(smem + k * 256 + ((cu * 16) ^ ((k & 3) << 6))) = v;
        }
        STAMP(0,1);
        __syncthreads();
        STAMP(0,2);
        const int r = (lane & 15) >> 2, cc = lane & 3;
#pragma unroll
        for (int tt = 0; tt < 4; ++tt) {
            f32x4 acc[2];
#pragma unroll
            for (int hf = 0; hf < 2; ++hf) {
                acc[hf] = (f32x4){0.f, 0.f, 0.f, 0.f};
#pragma unroll
                for (int ks = 0; ks < 4; ++ks) {
                    const int col = (64 * tt + 16 * cc + 8 * hf) ^ (r << 6) ^ ((q & 1) << 3);
                    const int k0 = 32 * ks + 8 * q + r;
                    const u32x2 a0 = tr_read(smem + k0 * 256 + col);
                    const u32x2 a1 = tr_read(smem + (k0 + 4) * 256 + col);
                    u32x4 a; a[0] = a0[0]; a[1] = a0[1]; a[2] = a1[0]; a[3] = a1[1];
                    acc[hf] = mfma16(a, sb[ks], acc[hf]);
                }
            }
            u32x4 o;
#pragma unroll
            for (int hf = 0; hf < 2; ++hf) {
                float y[4];
#pragma unroll
                for (int i = 0; i < 4; ++i) {
                    const float x = rbf16(acc[hf][i]);
                    y[i] = x * __builtin_amdgcn_rcpf(1.0f + __expf(-x));
                }
                o[2 * hf] = pack2(y[0], y[1]); o[2 * hf + 1] = pack2(y[2], y[3]);
            }
            if (node < N) *(u32x4*)(h + (size_t)node * 128 + 32 * tt + 8 * q) = o;
            STAMP(0,3+tt);
        }
    } else {
        const int T = (b - G - nNodeBlocks) * PRE_T + t;
        const int fi = T >> 6;
        const int ww = fi / 12, gg = (fi % 12) >> 2, ks = fi & 3;
        const int c = lane & 15, q = lane >> 4;
        u32 v[8];
#pragma unroll
        for (int j = 0; j < 8; ++j) v[j] = w2[(size_t)(32 * ks + 8 * q + j) * 384 + 128 * gg + 16 * ww + c];
        u32x4 o;
#pragma unroll
        for (int j = 0; j < 4; ++j) o[j] = v[2 * j] | (v[2 * j + 1] << 16);
        w2f[T] = o;
        if (ks == 0) {
            u32 r[4];
#pragma unroll
            for (int k = 0; k < 4; ++k) r[k] = wrbf[k * 384 + 128 * gg + 16 * ww + c];
            u32x2 p; p[0] = q < 2 ? (r[0] | (r[1] << 16)) : 0u; p[1] = q < 2 ? (r[2] | (r[3] << 16)) : 0u;
            wrf[(ww * 3 + gg) * 64 + lane] = p;
        }
    }
}

#define UNPACK_MFMA 0
#define RING_LOAD0(base, o0, o1)                                                                             \
    asm volatile("global_load_dwordx4 v[240:243], %0, %2\n\tglobal_load_dwordx4 v[244:247], %1, %2" ::"v"(o0), "v"(o1), "s"(base) \
                 : "memory", "v240", "v241", "v242", "v243", "v244", "v245", "v246", "v247")
#define RING_LOAD1(base, o0, o1)                                                                             \
    asm volatile("global_load_dwordx4 v[248:251], %0, %2\n\tglobal_load_dwordx4 v[252:255], %1, %2" ::"v"(o0), "v"(o1), "s"(base) \
                 : "memory", "v248", "v249", "v250", "v251", "v252", "v253", "v254", "v255")
#define RING_WRITE0(la) asm volatile("ds_write_b128 %0, v[240:243]\n\tds_write_b128 %0, v[244:247] offset:1040" ::"v"(la) : "memory")
#define RING_WRITE1(la) asm volatile("ds_write_b128 %0, v[248:251]\n\tds_write_b128 %0, v[252:255] offset:1040" ::"v"(la) : "memory")
DI void dma16c(const void* gsrc, u32 lds_base, int rem) {
    asm volatile("s_cmp_lt_i32 %2, 1\n\ts_cbranch_scc1 1f\n\ts_mov_b32 m0, %1\n\ts_nop 0\n\tglobal_load_lds_dwordx4 %0, off\n1:"
                 ::"v"(gsrc), "s"(lds_base), "s"(rem) : "memory", "scc");
}
#define SB() __builtin_amdgcn_sched_barrier(0)
DI void astore16nt(u32x4 d, void* base, u32 off) {
    asm volatile("global_store_dwordx4 %0, %1, %2 nt\n\ts_nop 1" ::"v"(off), "v"(d), "s"(base) : "memory");
}
DI u32x4 onehot(u32x2 dA, u32x2 dB, u32 tg, u32 kOne) {
    u32x4 S;
    const u32 kNeg = 0x80008000u;
    asm volatile(
        "v_xor_b32 %0, %4, %8\n\tv_xor_b32 %1, %5, %8\n\tv_xor_b32 %2, %6, %8\n\tv_xor_b32 %3, %7, %8\n\t"
        "v_pk_mad_i16 %0, %0, %9, %10 clamp\n\tv_pk_mad_i16 %1, %1, %9, %10 clamp\n\t"
        "v_pk_mad_i16 %2, %2, %9, %10 clamp\n\tv_pk_mad_i16 %3, %3, %9, %10 clamp\n\ts_nop 1"
        : "=&v"(S[0]), "=&v"(S[1]), "=&v"(S[2]), "=&v"(S[3])
        : "v"(dA[0]), "v"(dA[1]), "v"(dB[0]), "v"(dB[1]), "v"(tg), "s"(kNeg), "v"(kOne));
    return S;
}

__global__ __launch_bounds__(512, 2) __attribute__((amdgpu_num_vgpr(240))) void main_kernel(const u16* __restrict__ sv, const u16* __restrict__ pos,
                                                      const int* __restrict__ src, const int* __restrict__ dst,
                                                      const u16* __restrict__ freqs, const u16* __restrict__ h,
                                                      const u32x4* __restrict__ w2f, const u32x2* __restrict__ wrf,
                                                      const u32* __restrict__ bins, u16* __restrict__ out, int N, int E,
                                                      int NT) {
    extern __shared__ __attribute__((aligned(16))) char smem[];
    const int t = threadIdx.x, lane = t & 63;
    const int w = __builtin_amdgcn_readfirstlane(t >> 6);
    const int b = blockIdx.x;
    const int tile = (b & 7) * (TILES / 8) + (b >> 3);
    const int c16 = lane & 15, q = lane >> 4;
    const int node0 = tile * NT;
    const int nvalid = min(NT, N - node0);
    if (nvalid <= 0) return;
    const u32 lbase = (u32)(size_t)(lds_char*)smem;
    MST_DECL;
    MST(0);

    u32x4 bw;
    aload16(bw, (const char*)bins + (size_t)tile * (G * 64), (u32)t * 16u);
    u32 pdv;
    aload2(pdv, pos + (size_t)node0 * 3, (u32)min(t, 3 * nvalid - 1) * 2u);
    u32x4 W2[3][4];
#pragma unroll
    for (int gg = 0; gg < 3; ++gg)
#pragma unroll
        for (int ks = 0; ks < 4; ++ks) aload16(W2[gg][ks], w2f, (u32)((w * 12 + gg * 4 + ks) * 64 + lane) * 16u);
    u32x2 WR[3];
#pragma unroll
    for (int gg = 0; gg < 3; ++gg) aload8(WR[gg], wrf, (u32)((w * 3 + gg) * 64 + lane) * 8u);
    const char* const rbase = (const char*)sv + (size_t)node0 * 1024;
#define RES_OFF(rb, i) ((u32)min((rb) * 16 + 2 * w + (i), nvalid - 1) * 1024u + (u32)lane * 16u)
    u32x4 T0[2], T1[2];
    aload16(T0[0], rbase, RES_OFF(0, 0)); aload16(T0[1], rbase, RES_OFF(0, 1));
    aload16(T1[0], rbase, RES_OFF(1, 0)); aload16(T1[1], rbase, RES_OFF(1, 1));
    const float fr0 = bf2f(freqs[0]), fr1 = bf2f(freqs[1]), fr2 = bf2f(freqs[2]), fr3 = bf2f(freqs[3]);

    u32* const l_tmp = (u32*)(smem + L_STG);
    u32* const l_fin = (u32*)(smem + L_FIN);
    u32* const l_hist = (u32*)(smem + L_HIST);
    u32* const l_start = (u32*)(smem + L_START);
    float* const l_posd = (float*)(smem + L_POSD);
    u32* const l_misc = (u32*)(smem + L_MISC);
    u16* const l_dl = (u16*)(smem + L_DL);
    float* const l_rh = (float*)(smem + L_RH);

    if (t < 128) l_hist[t] = 0;
    if (t == 128) l_misc[0] = 0;
    lds_barrier();

    VMWAIT(19);
    asm volatile("" : "+v"(bw), "+v"(pdv));
    MST(1);
    if (t < 3 * nvalid) l_posd[t] = bf2f(pdv);
    const u32 cnt = (u32)__builtin_amdgcn_update_dpp(0, (int)bw[0], 0x00, 0xf, 0xf, false);
    if (cnt > 15u) l_misc[0] = 1;
    u32 rk[4];
    bool vl[4];
#pragma unroll
    for (int i = 0; i < 4; ++i) {
        const u32 widx = 4 * (t & 3) + i;
        vl[i] = widx >= 1 && widx <= cnt && cnt <= 15u;
        rk[i] = 0;
        if (vl[i]) rk[i] = atomicAdd(&l_hist[bw[i] >> 24], 1u);
    }
    lds_barrier();
    u32 starts;
    int total;
    {
        const u32x2 hh = *(const u32x2*)(l_hist + 2 * lane);
        const u32 inc = wave_incl_scan(hh[0] + hh[1]);
        const u32 s0 = inc - hh[0] - hh[1], s1 = s0 + hh[0];
        starts = s0 | (s1 << 16);
        total = __builtin_amdgcn_readlane((int)inc, 63);
        if (w == 0) { u32x2 sx; sx[0] = s0; sx[1] = s1; *(u32x2*)(l_start + 2 * lane) = sx; if (lane == 63) l_start[128] = inc; }
    }
    const bool slow = __builtin_amdgcn_readfirstlane((int)l_misc[0]) != 0;
#pragma unroll
    for (int i = 0; i < 4; ++i) {
        const u32 loc = (bw[i] >> 24) & 127u;
        const u32 p = (u32)__builtin_amdgcn_ds_bpermute((int)((loc >> 1) << 2), (int)starts);
        const u32 st = (loc & 1) ? (p >> 16) : (p & 0xffffu);
        if (vl[i]) l_tmp[st + rk[i]] = bw[i];
    }
    if (t >= 480) l_fin[total + t - 480] = 0;
    lds_barrier();
    for (int p = t; p < total; p += 512) {
        const u32 rec = l_tmp[p];
        const u32 loc = rec >> 24;
        const int s = (int)l_start[loc], e = (int)l_start[loc + 1];
        int rank = 0;
        for (int j = s; j < e; j += 2) {
            const u32 k0 = l_tmp[j], k1 = l_tmp[j + 1];
            rank += (k0 < rec || (k0 == rec && j < p)) ? 1 : 0;
            rank += (j + 1 < e && (k1 < rec || (k1 == rec && j + 1 < p))) ? 1 : 0;
        }
        l_fin[s + rank] = rec;
    }
    lds_barrier();
    MST(2);

    f32x4 acc[4];
#pragma unroll
    for (int x = 0; x < 4; ++x) acc[x] = (f32x4){0.f, 0.f, 0.f, 0.f};
    int cur = 0;
    int sto = 0;
    int epoch = 0, eS0 = -1, eS1 = -1;
    int issued = 0;
    int mR0 = 0, mR1 = 0;

    const u32 dshift = lane < 16 ? 8u : 10u;
    const char* const dbase = (lane < 16 ? (const char*)h : (const char*)sv) + lane * 16;
    char* const obase = (char*)out + (size_t)node0 * 1024;
    const u32 kOne = 0x3F803F80u;
    u32x2 ident;
    ident[0] = (4 * q == c16 ? 0x3F80u : 0u) | (4 * q + 1 == c16 ? 0x3F800000u : 0u);
    ident[1] = (4 * q + 2 == c16 ? 0x3F80u : 0u) | (4 * q + 3 == c16 ? 0x3F800000u : 0u);
    u32 tg = (u32)(2 * c16) * 0x10001u;

#define STORE_ONE(d0_, d1_)                                                                                 \
    do {                                                                                                    \
        const u32 la_ = lbase + L_STG + (u32)((sto & 1) * STAGE_B + 2 * w * SPITCH) + (u32)lane * 16u;      \
        asm volatile("" : "+v"(d0_), "+v"(d1_));                                                            \
        if (sto < 5) {                                                                                      \
            if (sto & 1) {                                                                                  \
                wait_vm(issued - mR1);                                                                      \
                RING_WRITE1(la_);                                                                           \
                if (sto < 3) {                                                                              \
                    RING_LOAD1(rbase, RES_OFF(sto + 4, 0), RES_OFF(sto + 4, 1));                            \
                    issued += 2; mR1 = issued;                                                              \
                }                                                                                           \
                eS1 = epoch;                                                                                \
            } else {                                                                                        \
                wait_vm(issued - mR0);                                                                      \
                RING_WRITE0(la_);                                                                           \
                if (sto < 3) {                                                                              \
                    RING_LOAD0(rbase, RES_OFF(sto + 4, 0), RES_OFF(sto + 4, 1));                            \
                    issued += 2; mR0 = issued;                                                              \
                }                                                                                           \
                eS0 = epoch;                                                                                \
            }                                                                                               \
        }                                                                                                   \
        const int row_ = sto * 16 + 2 * w;                                                                  \
        const u32 oo_ = (u32)row_ * 1024u + (u32)lane * 16u;                                                \
        if (row_ < nvalid) { astore16nt(d0_, obase, oo_); ++issued; }                                       \
        if (row_ + 1 < nvalid) { astore16nt(d1_, obase, oo_ + 1024u); ++issued; }                           \
        ++sto;                                                                                              \
    } while (0)
#define STG_ROWS(rb) (smem + L_STG + ((rb) & 1) * STAGE_B + 2 * w * SPITCH + lane * 16)
#define EPOCH_BARRIER()                                                                                     \
    do {                                                                                                    \
        const int lim_ = cur;                                                                               \
        lds_barrier();                                                                                      \
        ++epoch;                                                                                            \
        while (sto < lim_) {                                                                                \
            u32x4 e0_ = *(const u32x4*)STG_ROWS(sto), e1_ = *(const u32x4*)(STG_ROWS(sto) + SPITCH);        \
            STORE_ONE(e0_, e1_);                                                                            \
        }                                                                                                   \
    } while (0)
#define READY() (sto >= cur - 1 && epoch > ((cur & 1) ? eS1 : eS0))
#define RES_READ(rt)                                                                                        \
    _Pragma("unroll") for (int x_ = 0; x_ < 4; ++x_)                                                        \
        rt[x_] = tr_read(smem + L_STG + (cur & 1) * STAGE_B + 32 * w + (4 * q + (c16 >> 2)) * SPITCH + 8 * (c16 & 3) + 256 * x_)
#define FLUSH_CUR(rt, have)                                                                                 \
    do {                                                                                                    \
        if (!(have)) {                                                                                      \
            while (!READY()) EPOCH_BARRIER();                                                               \
            RES_READ(rt);                                                                                   \
        }                                                                                                   \
        char* sb_ = smem + L_STG + (cur & 1) * STAGE_B + 32 * w;                                            \
        _Pragma("unroll") for (int x_ = 0; x_ < 4; ++x_) {                                                  \
            const f32x4 f_ = mfma16k16(rt[x_], ident, acc[x_]);                                             \
            u32x2 o_; o_[0] = pack2(f_[0], f_[1]); o_[1] = pack2(f_[2], f_[3]);                             \
            *(u32x2*)(sb_ + c16 * SPITCH + 8 * q + 256 * x_) = o_;                                          \
        }                                                                                                   \
        ++cur; tg += 32u * 0x10001u;                                                                        \
    } while (0)
#define SEG(rt, have)                                                                                       \
    do {                                                                                                    \
        int endc_ = __builtin_amdgcn_readlane((int)rbtab, cur);                                             \
        if (endc_ > sEb) {                                                                                  \
            const u32x4 S_ = onehot(dA, dB, tg, kOne);                                                      \
            _Pragma("unroll") for (int x_ = 0; x_ < 4; ++x_) acc[x_] = mfma16(A[x_], S_, acc[x_]);          \
        }                                                                                                   \
        bool have_ = (have);                                                                                \
        while (endc_ < sE1) {                                                                               \
            FLUSH_CUR(rt, have_);                                                                           \
            have_ = false;                                                                                  \
            endc_ = __builtin_amdgcn_readlane((int)rbtab, cur);                                             \
            const u32x4 S_ = onehot(dA, dB, tg, kOne);                                                      \
            _Pragma("unroll") for (int x_ = 0; x_ < 4; ++x_)                                                \
                acc[x_] = mfma16(A[x_], S_, (f32x4){0.f, 0.f, 0.f, 0.f});                                   \
        }                                                                                                   \
    } while (0)

    int W0 = 0;
    int slowR = 0, slowE = 0;
    bool first = true;
    for (;;) {
        int nrec;
        u32 rbtab;
        const u32* list;
        if (!slow) {
            if (W0 >= total) break;
            nrec = min(CAP, total - W0);
            list = l_fin + W0;
            const u32 nx = (u32)__builtin_amdgcn_ds_bpermute(((lane & 7) + 1) * 32, (int)starts) & 0xffffu;
            rbtab = (u32)min(max((int)nx - W0, 0), nrec);
            W0 += CAP;
            if (!first) lds_barrier();
        } else {
            int n = 0;
            lds_barrier();
            while (slowR < 7) {
                if (slowE >= E) {
                    ++slowR; slowE = 0;
                    if (n > 0) { --slowR; slowE = E; break; }
                    continue;
                }
                const int e = slowE + t;
                int di = -1, si = 0;
                if (e < E) { di = dst[e]; si = src[e]; }
                asm volatile("" : "+v"(di), "+v"(si));
                const int loc = di - node0;
                const bool m = e < E && loc >= 0 && loc < nvalid && (loc >> 4) == slowR;
                const u64 bal = __ballot(m);
                const int pw = __builtin_amdgcn_mbcnt_hi((u32)(bal >> 32), __builtin_amdgcn_mbcnt_lo((u32)bal, 0));
                if (lane == 0) l_hist[w] = (u32)__popcll(bal);
                lds_barrier();
                int basep = 0, all = 0;
#pragma unroll
                for (int i = 0; i < 8; ++i) { const int c = (int)l_hist[i]; basep += i < w ? c : 0; all += c; }
                all = __builtin_amdgcn_readfirstlane(all);
                const bool fits = n + all <= CAP;
                if (fits && m) l_fin[n + basep + pw] = ((u32)loc << 24) | (u32)si;
                lds_barrier();
                if (!fits) break;
                n += all; slowE += 512;
            }
            if (n == 0) {
                if (slowR >= 7) break;
            }
            if (t < 32) l_fin[n + t] = 0;
            lds_barrier();
            nrec = n;
            list = l_fin;
            rbtab = lane < slowR ? 0u : (u32)n;
            if (slowE >= E) { ++slowR; slowE = 0; }
            if (n == 0) continue;
        }
        nrec = __builtin_amdgcn_readfirstlane(nrec);
        const int nch = (nrec + 31) >> 5;
        const u32 rec = list[min(t, nrec - 1)];
        const u32 rsrc = rec & 0xffffffu;
        u32 px, py, pz;
        aload2(px, pos, rsrc * 6u);
        aload2(py, pos + 1, rsrc * 6u);
        aload2(pz, pos + 2, rsrc * 6u);
        issued += 3;
        const int mPos = issued;
        int mA, mB, mC;
        u32 bA = lbase + L_BUF, bB = lbase + L_BUF + CHUNK_B, bC = lbase + L_BUF + 2 * CHUNK_B;
        const u32* const listw = list + 4 * w;
#define DMA_ROW(r4, i_, buf, en) dma16c(dbase + ((r4)[i_] << dshift), (buf) + (u32)(4 * w + (i_)) * PITCH, en)
        u32x4 nr4;
        {
            const u32x4 r0 = *(const u32x4*)listw, r1 = *(const u32x4*)(listw + 32);
            nr4 = *(const u32x4*)(listw + 64);
#pragma unroll
            for (int i = 0; i < 4; ++i) DMA_ROW(r0, i, bA, 1);
            issued += 4;
            mA = issued;
            const int rem1 = nch - 1, en1 = rem1 > 0 ? 1 : 0;
#pragma unroll
            for (int i = 0; i < 4; ++i) DMA_ROW(r1, i, bB, rem1);
            issued += 4 * en1;
            mB = issued;
            mC = issued;
        }
        MST(3);
        wait_vm(issued - mPos);
        asm volatile("" : "+v"(px), "+v"(py), "+v"(pz));
        MST(4);
        if (first) {
#pragma unroll
            for (int gg = 0; gg < 3; ++gg) {
                asm volatile("" : "+v"(WR[gg]));
#pragma unroll
                for (int ks = 0; ks < 4; ++ks) asm volatile("" : "+v"(W2[gg][ks]));
            }
            asm volatile("" : "+v"(T0[0]), "+v"(T0[1]), "+v"(T1[0]), "+v"(T1[1]));
            char* sp = smem + L_STG + 2 * w * SPITCH + lane * 16;
            *(u32x4*)sp = T0[0]; *(u32x4*)(sp + SPITCH) = T0[1];
            *(u32x4*)(sp + STAGE_B) = T1[0]; *(u32x4*)(sp + STAGE_B + SPITCH) = T1[1];
        }
        if (t < nch * 32) {
            u32x4 a = (u32x4){0u, 0u, 0u, 0u};
            float rh0 = 0.f, rh1 = 0.f, rh2 = 0.f;
            const u32 loc = rec >> 24;
            if (t < nrec) {
                const float rx = rbf16(bf2f(px) - l_posd[3 * loc]);
                const float ry = rbf16(bf2f(py) - l_posd[3 * loc + 1]);
                const float rz = rbf16(bf2f(pz) - l_posd[3 * loc + 2]);
                const float ss = rbf16(rbf16(rx * rx) + rbf16(ry * ry) + rbf16(rz * rz));
                const float d = rbf16(sqrtf(rbf16(ss + 1e-8f)));
                const float inv = __builtin_amdgcn_rcpf(d);
                rh0 = rbf16(rx * inv); rh1 = rbf16(ry * inv); rh2 = rbf16(rz * inv);
                const float i2pi = 0.15915494309189535f;
                float ang = rbf16(rbf16(3.14159265358979323846f * d) / 5.0f);
                float fc = rbf16(__builtin_amdgcn_cosf(__builtin_amdgcn_fractf(ang * i2pi)));
                fc = rbf16(0.5f * rbf16(fc + 1.0f));
                fc = d < 5.0f ? fc : 0.0f * d;
                float cf[4];
                const float fr[4] = {fr0, fr1, fr2, fr3};
#pragma unroll
                for (int k = 0; k < 4; ++k) {
                    const float arg = rbf16(d * fr[k]);
                    const float sn = rbf16(__builtin_amdgcn_sinf(__builtin_amdgcn_fractf(arg * i2pi)));
                    cf[k] = rbf16(sn * inv) * fc;
                }
                float hi[4], lo[4];
#pragma unroll
                for (int k = 0; k < 4; ++k) { hi[k] = rbf16(cf[k]); lo[k] = cf[k] - hi[k]; }
                a[0] = pack2(hi[0], hi[1]); a[1] = pack2(hi[2], hi[3]); a[2] = pack2(lo[0], lo[1]); a[3] = pack2(lo[2], lo[3]);
            }
            *(u32x4*)(smem + L_WFA + t * 16) = a;
            l_rh[t] = rh0; l_rh[CAP + t] = rh1; l_rh[2 * CAP + t] = rh2;
            l_dl[t] = t < nrec ? (u16)(2 * loc) : (u16)0x7FFEu;
        }
        if (first) {
            RING_LOAD0(rbase, RES_OFF(2, 0), RES_OFF(2, 1));
            issued += 2; mR0 = issued;
            RING_LOAD1(rbase, RES_OFF(3, 0), RES_OFF(3, 1));
            issued += 2; mR1 = issued;
            first = false;
        }
        MST(5);

        u32x4 A[4];
        u32x2 dA, dB;
        int sEb = 0, sE1 = 0;
#pragma unroll
        for (int x = 0; x < 4; ++x) A[x] = (u32x4){0u, 0u, 0u, 0u};
        dA = dB = (u32x2){0u, 0u};
        for (int ch = 0; ch < nch; ++ch) {
            if (ch < 10) MST(8+5*ch);
            wait_vm(issued - mA);
            if (ch < 10) MST(9+5*ch);
            lds_barrier();
            ++epoch;
            if (ch < 10) MST(10+5*ch);
            const char* B = smem + (bA - lbase);
            const int eb = ch * 32;
            const int stoTop = sto, curTop = cur;
            const bool pref = READY();
            const int rem = nch - ch - 2, en = rem > 0 ? 1 : 0;
            u32x2 wa[2], rt[4];
            u32x4 a0[4], a1[4];
#pragma unroll
            for (int blk = 0; blk < 2; ++blk) wa[blk] = *(const u32x2*)(smem + L_WFA + (eb + 16 * blk + c16) * 16 + (q & 1) * 8);
            RES_READ(rt);
#pragma unroll
            for (int ks = 0; ks < 4; ++ks) a0[ks] = *(const u32x4*)(B + c16 * PITCH + ks * 64 + q * 16);
            u32x4 sd0 = *(const u32x4*)STG_ROWS(sto), sd1 = *(const u32x4*)(STG_ROWS(sto) + SPITCH);
#pragma unroll
            for (int ks = 0; ks < 4; ++ks) a1[ks] = *(const u32x4*)(B + (16 + c16) * PITCH + ks * 64 + q * 16);
            SB();
            if (ch >= 1) SEG(rt, pref);
            if (ch < 10) MST(11+5*ch);
            SB();
            u32x2 vv[3];
            f32x4 rh[3];
#define V_READ(blk)                                                                                         \
    _Pragma("unroll") for (int x = 0; x < 3; ++x) {                                                         \
        vv[x] = tr_read(B + (16 * (blk) + 4 * q + (c16 >> 2)) * PITCH + 256 * (1 + x) + 32 * w + 8 * (c16 & 3)); \
        rh[x] = *(const f32x4*)(l_rh + x * CAP + eb + 16 * (blk) + 4 * q);                                  \
    }
#define MSG_HEAD(blk)                                                                                       \
    do {                                                                                                    \
        const f32x4 ms = Z[0] * Wf[0];                                                                      \
        mvv = Z[1] * Wf[1]; mvr = Z[2] * Wf[2];                                                             \
        A[0][2 * (blk)] = pack2(ms[0], ms[1]); A[0][2 * (blk) + 1] = pack2(ms[2], ms[3]);                   \
    } while (0)
#define MSG_V(blk, x)                                                                                       \
    do {                                                                                                    \
        const f32x4 vf = VUNPACK(vv[x]);                                                                    \
        const f32x4 u = mvv * vf + mvr * rh[x];                                                             \
        A[1 + (x)][2 * (blk)] = pack2(u[0], u[1]); A[1 + (x)][2 * (blk) + 1] = pack2(u[2], u[3]);           \
    } while (0)
#define VUNPACK(v) ((f32x4){lo16((v)[0]), hi16((v)[0]), lo16((v)[1]), hi16((v)[1])})
            f32x4 Z[3], Wf[3], Y[3], Wg[3], mvv, mvr;
            V_READ(0);
#pragma unroll
            for (int gg = 0; gg < 3; ++gg) Z[gg] = mfma16(a0[0], W2[gg][0], (f32x4){0.f, 0.f, 0.f, 0.f});
#pragma unroll
            for (int gg = 0; gg < 3; ++gg) Z[gg] = mfma16(a0[1], W2[gg][1], Z[gg]);
            SB();
            DMA_ROW(nr4, 0, bC, rem);
            issued += en;
            SB();
#pragma unroll
            for (int gg = 0; gg < 3; ++gg) Wf[gg] = mfma16k16(wa[0], WR[gg], (f32x4){0.f, 0.f, 0.f, 0.f});
#pragma unroll
            for (int gg = 0; gg < 3; ++gg) Z[gg] = mfma16(a0[2], W2[gg][2], Z[gg]);
#pragma unroll
            for (int gg = 0; gg < 3; ++gg) Z[gg] = mfma16(a0[3], W2[gg][3], Z[gg]);
            SB();
            DMA_ROW(nr4, 1, bC, rem);
            issued += en;
            SB();
            if (sto == stoTop && stoTop < curTop) STORE_ONE(sd0, sd1);
            if (ch < 10) MST(12+5*ch);
            SB();
#pragma unroll
            for (int gg = 0; gg < 3; ++gg) Y[gg] = mfma16(a1[0], W2[gg][0], (f32x4){0.f, 0.f, 0.f, 0.f});
            MSG_HEAD(0);
            SB();
#pragma unroll
            for (int gg = 0; gg < 3; ++gg) Y[gg] = mfma16(a1[1], W2[gg][1], Y[gg]);
            MSG_V(0, 0);
            SB();
            DMA_ROW(nr4, 2, bC, rem);
            issued += en;
            SB();
#pragma unroll
            for (int gg = 0; gg < 3; ++gg) Y[gg] = mfma16(a1[2], W2[gg][2], Y[gg]);
            MSG_V(0, 1);
            SB();
#pragma unroll
            for (int gg = 0; gg < 3; ++gg) Y[gg] = mfma16(a1[3], W2[gg][3], Y[gg]);
            MSG_V(0, 2);
            SB();
            V_READ(1);
            dA = *(const u32x2*)(l_dl + eb + 4 * q); dB = *(const u32x2*)(l_dl + eb + 16 + 4 * q);
#pragma unroll
            for (int gg = 0; gg < 3; ++gg) Wg[gg] = mfma16k16(wa[1], WR[gg], (f32x4){0.f, 0.f, 0.f, 0.f});
            SB();
            DMA_ROW(nr4, 3, bC, rem);
            issued += en; mC = issued;
            nr4 = *(const u32x4*)(listw + (ch + 3) * 32);
            SB();
#pragma unroll
            for (int gg = 0; gg < 3; ++gg) { Z[gg] = Y[gg]; Wf[gg] = Wg[gg]; }
            MSG_HEAD(1);
            MSG_V(1, 0); MSG_V(1, 1); MSG_V(1, 2);
            sEb = eb; sE1 = min(eb + 32, nrec);
            mA = mB; mB = mC;
            const u32 tb = bA; bA = bB; bB = bC; bC = tb;
        }
        {
            u32x2 rt[4];
            SEG(rt, false);
        }
    }
    MST(6);
    if (first) {
        RING_LOAD0(rbase, RES_OFF(2, 0), RES_OFF(2, 1));
        issued += 2; mR0 = issued;
        RING_LOAD1(rbase, RES_OFF(3, 0), RES_OFF(3, 1));
        issued += 2; mR1 = issued;
        VMWAIT(4);
        asm volatile("" : "+v"(T0[0]), "+v"(T0[1]), "+v"(T1[0]), "+v"(T1[1]));
        char* sp = smem + L_STG + 2 * w * SPITCH + lane * 16;
        *(u32x4*)sp = T0[0]; *(u32x4*)(sp + SPITCH) = T0[1];
        *(u32x4*)(sp + STAGE_B) = T1[0]; *(u32x4*)(sp + STAGE_B + SPITCH) = T1[1];
        lds_barrier();
        ++epoch;
    }
    while (cur < 7) {
        u32x2 rt[4];
        FLUSH_CUR(rt, false);
#pragma unroll
        for (int x = 0; x < 4; ++x) acc[x] = (f32x4){0.f, 0.f, 0.f, 0.f};
    }
    EPOCH_BARRIER();
    MST(7);
    MST_OUT();
}

extern "C" void kernel_launch(void* const* d_in, const int* in_sizes, int n_in, void* d_out, int out_size, void* d_ws,
                              size_t ws_size, hipStream_t stream) {
    const u16* sv = (const u16*)d_in[0];
    const u16* pos = (const u16*)d_in[1];
    const int* src = (const int*)d_in[2];
    const int* dst = (const int*)d_in[3];
    const u16* freqs = (const u16*)d_in[4];
    const u16* w1 = (const u16*)d_in[5];
    const u16* w2 = (const u16*)d_in[6];
    const u16* wrbf = (const u16*)d_in[7];
    u16* out = (u16*)d_out;
    const int N = in_sizes[0] / 512;
    const int E = in_sizes[2];
    const int NT = (N + TILES - 1) / TILES;
    const int EPB = ((E + G - 1) / G + 3) / 4 * 4;
    const u32 ntmagic = (u32)((0x100000000ull + NT - 1) / NT);
    const int nNodeBlocks = (N + NODES_PB - 1) / NODES_PB;

    char* ws = (char*)d_ws;
    size_t off = 0;
    u16* h = (u16*)(ws + off); off += ((size_t)N * 256 + 255) / 256 * 256;
    u32x4* w2f = (u32x4*)(ws + off); off += 6144 * 16;
    u32x2* wrf = (u32x2*)(ws + off); off += 24 * 64 * 8;
    u32* bins = (u32*)(ws + off);

    const int posBytes = ((N * 6 + 15) / 16) * 16;
    const int preLds = posBytes + TILES * 4 + 64;
    (void)hipFuncSetAttribute((const void*)pre_kernel, hipFuncAttributeMaxDynamicSharedMemorySize, preLds);
    (void)hipFuncSetAttribute((const void*)main_kernel, hipFuncAttributeMaxDynamicSharedMemorySize, MAIN_LDS);
    pre_kernel<<<G + nNodeBlocks + W2_BLOCKS, PRE_T, preLds, stream>>>(sv, pos, src, dst, w1, w2, wrbf, h, w2f, wrf, bins, N, E,
                                                                       EPB, NT, ntmagic, nNodeBlocks);
    main_kernel<<<TILES, 512, MAIN_LDS, stream>>>(sv, pos, src, dst, freqs, h, w2f, wrf, bins, out, N, E, NT);
}
```

```cpp
#include <hip/hip_runtime.h>
#include <stdint.h>

typedef unsigned short u16;
typedef unsigned int u32;
typedef unsigned long long u64;
typedef __attribute__((ext_vector_type(8))) __bf16 bf16x8;
typedef __attribute__((ext_vector_type(2))) __bf16 bf16x2;
typedef __attribute__((ext_vector_type(4))) float f32x4;
typedef __attribute__((ext_vector_type(4))) u32 u32x4;
typedef __attribute__((ext_vector_type(2))) u32 u32x2;
typedef __attribute__((ext_vector_type(4))) int i32x4;
typedef __attribute__((ext_vector_type(4))) short s16x4;
typedef __attribute__((ext_vector_type(2))) u16 u16x2;
typedef __attribute__((address_space(3))) char lds_char;

#define DI __device__ __forceinline__
#define STAMP(k,i)
#define STAMPW(k,i,wv)
#define MST(i)
#define MSTI(i)
#define MST_DECL
#define MST_OUT()

constexpr int TILES = 256;
constexpr int G = 128;
constexpr int PRE_T = 1024;
constexpr int NODES_PB = 256;
constexpr int W2_BLOCKS = 6;
constexpr int CAP = 512;
constexpr int RC = 2048;
constexpr int PITCH = 1056;
constexpr int CHUNK_B = 32 * PITCH;
constexpr int SPITCH = 1040;
constexpr int STAGE_B = 16 * SPITCH;

constexpr int L_BUF = 0;
constexpr int L_STG = 3 * CHUNK_B;
constexpr int L_WFA = L_STG + 2 * STAGE_B;
constexpr int L_RH = L_WFA + CAP * 16;
constexpr int L_DL = L_RH + 3 * CAP * 4;
constexpr int L_FIN = L_DL + CAP * 2;
constexpr int L_HIST = L_FIN + RC * 4;
constexpr int L_START = L_HIST + 512;
constexpr int L_POSD = L_START + 544;
constexpr int L_MISC = L_POSD + 1344;
constexpr int MAIN_LDS = L_MISC + 64;
static_assert(MAIN_LDS <= 163840, "lds");

DI float bf2f(u32 b) { return __uint_as_float(b << 16); }
DI float rbf16(float x) { return (float)(__bf16)x; }
DI u32 pack2(float a, float b) { bf16x2 v; v[0] = (__bf16)a; v[1] = (__bf16)b; return __builtin_bit_cast(u32, v); }
DI float lo16(u32 p) { return __uint_as_float(p << 16); }
DI float hi16(u32 p) { return __uint_as_float(p & 0xffff0000u); }

DI u32 wave_incl_scan(u32 v) {
    v += (u32)__builtin_amdgcn_update_dpp(0, (int)v, 0x111, 0xf, 0xf, false);
    v += (u32)__builtin_amdgcn_update_dpp(0, (int)v, 0x112, 0xf, 0xf, false);
    v += (u32)__builtin_amdgcn_update_dpp(0, (int)v, 0x114, 0xf, 0xf, false);
    v += (u32)__builtin_amdgcn_update_dpp(0, (int)v, 0x118, 0xf, 0xf, false);
    v += (u32)__builtin_amdgcn_update_dpp(0, (int)v, 0x142, 0xa, 0xf, false);
    v += (u32)__builtin_amdgcn_update_dpp(0, (int)v, 0x143, 0xc, 0xf, false);
    return v;
}

DI void dma16(const void* gsrc, u32 lds_base) {
    asm volatile("s_mov_b32 m0, %1\n\ts_nop 0\n\tglobal_load_lds_dwordx4 %0, off" ::"v"(gsrc), "s"(lds_base) : "memory");
}
DI void lds_barrier() {
    asm volatile("s_waitcnt lgkmcnt(0)" ::: "memory");
    __builtin_amdgcn_s_barrier();
    asm volatile("" ::: "memory");
}
#define VMWAIT(n) asm volatile("s_waitcnt vmcnt(" #n ")" ::: "memory")
DI void wait_vm(int n) {
    if (n >= 8) {
        if (n >= 12) { if (n >= 16) VMWAIT(16); else if (n >= 14) VMWAIT(14); else VMWAIT(12); }
        else { if (n >= 10) VMWAIT(10); else VMWAIT(8); }
    } else if (n >= 4) {
        if (n >= 6) VMWAIT(6); else VMWAIT(4);
    } else {
        if (n >= 2) VMWAIT(2); else VMWAIT(0);
    }
}
typedef __attribute__((ext_vector_type(4))) short v4i16_t;
DI u32x2 tr_read(const char* p) {
    v4i16_t r = __builtin_amdgcn_ds_read_tr16_b64_v4i16((__attribute__((address_space(3))) v4i16_t*)p);
    return __builtin_bit_cast(u32x2, r);
}
DI f32x4 mfma16(u32x4 a, u32x4 b, f32x4 c) {
    return __builtin_amdgcn_mfma_f32_16x16x32_bf16(__builtin_bit_cast(bf16x8, a), __builtin_bit_cast(bf16x8, b), c, 0, 0, 0);
}
DI f32x4 mfma16k16(u32x2 a, u32x2 b, f32x4 c) {
    return __builtin_amdgcn_mfma_f32_16x16x16bf16_1k(__builtin_bit_cast(s16x4, a), __builtin_bit_cast(s16x4, b), c, 0, 0, 0);
}
DI void aload16(u32x4& d, const void* base, u32 off) {
    asm volatile("global_load_dwordx4 %0, %1, %2" : "=v"(d) : "v"(off), "s"(base) : "memory");
}
DI void aload8(u32x2& d, const void* base, u32 off) {
    asm volatile("global_load_dwordx2 %0, %1, %2" : "=v"(d) : "v"(off), "s"(base) : "memory");
}
DI void aload2(u32& d, const void* base, u32 off) {
    asm volatile("global_load_ushort %0, %1, %2" : "=v"(d) : "v"(off), "s"(base) : "memory");
}

#define WT_H 3
#define WT_B 0
#define HFLAGS " sc1"
#define BFLAGS ""
DI void hstore16(u32x4 d, void* p) { asm volatile("global_store_dwordx4 %0, %1, off" HFLAGS "\n\ts_nop 1" ::"v"(p), "v"(d) : "memory"); }
DI void bstore4(u32 d, void* p) { asm volatile("global_store_dword %0, %1, off" BFLAGS "\n\ts_nop 1" ::"v"(p), "v"(d) : "memory"); }

__global__ __launch_bounds__(PRE_T) void pre_kernel(const u16* __restrict__ sv, const u16* __restrict__ pos,
                                                    const int* __restrict__ src, const int* __restrict__ dst,
                                                    const u16* __restrict__ w1, const u16* __restrict__ w2,
                                                    const u16* __restrict__ wrbf, u16* __restrict__ h,
                                                    u32x4* __restrict__ w2f, u32x2* __restrict__ wrf, u32* __restrict__ bins,
                                                    int N, int E, int EPB, int NT, u32 ntmagic, int nNodeBlocks) {
    extern __shared__ __attribute__((aligned(16))) char smem[];
    const int t = threadIdx.x, lane = t & 63;
    const int w = __builtin_amdgcn_readfirstlane(t >> 6);
    const int b = blockIdx.x;
    STAMP(0,0);
    if (b < G) {
        const int g = (b & 7) * (G / 8) + (b >> 3);
        const int posUnits = (N * 6 + 15) >> 4;
        const int posBytes = posUnits * 16;
        u32* hist = (u32*)(smem + posBytes);
        const int e0 = g * EPB + t * 4;
        const int eEnd = min(E, (g + 1) * EPB);
        const int e0c = min(e0, E - 4);
        u32x4 s4, d4;
        aload16(s4, src, (u32)e0c * 4u);
        aload16(d4, dst, (u32)e0c * 4u);
        const u32 lbase = (u32)(size_t)(lds_char*)smem;
        for (int u0 = w * 64; u0 < posUnits; u0 += PRE_T) {
            const int u = u0 + lane;
            if ((u + 1) * 16 <= N * 6) dma16((const char*)pos + (size_t)u * 16, lbase + (u32)u0 * 16u);
        }
        if (t < TILES) hist[t] = 0;
        {
            const int tail0 = (N * 6) & ~15;
            if (t < ((N * 6 - tail0) >> 1)) ((u16*)(smem + tail0))[t] = pos[(tail0 >> 1) + t];
        }
        STAMP(0,1);
        VMWAIT(0);
        STAMP(0,2);
        asm volatile("" : "+v"(s4), "+v"(d4));
        lds_barrier();
        STAMP(0,3);
        const u32* posw = (const u32*)smem;
#pragma unroll
        for (int i = 0; i < 4; ++i) {
            const bool ok = e0c + i >= e0 && e0c + i < eEnd;
            const u32 si = s4[i], di = d4[i];
            const u32 as = (3 * si) >> 1, ad = (3 * di) >> 1;
            const u32 as0 = posw[as], as1 = posw[as + 1];
            const u32 ad0 = posw[ad], ad1 = posw[ad + 1];
            const u32 shs = (si & 1) * 16, shd = (di & 1) * 16;
            const u32 sxy = __builtin_amdgcn_alignbit(as1, as0, shs), dxy = __builtin_amdgcn_alignbit(ad1, ad0, shd);
            const float rx = lo16(sxy) - lo16(dxy);
            const float ry = hi16(sxy) - hi16(dxy);
            const float rz = lo16(as1 >> shs) - lo16(ad1 >> shd);
            const float ss = rx * rx + ry * ry + rz * rz;
            const bool drop = (__float_as_uint(ss) - 0x41CA0000u) <= (0x7E000000u - 0x41CA0000u);
            if (ok && !drop) {
                const u32 tile = __umulhi(di, ntmagic);
                const u32 loc = di - tile * (u32)NT;
                const u32 rank = atomicAdd(&hist[tile], 1u);
                if (rank < 15u) bstore4((loc << 24) | si, &bins[((size_t)tile * G + g) * 16 + 1 + rank]);
            }
        }
        STAMP(0,4);
        lds_barrier();
        STAMP(0,5);
        if (t < TILES) bstore4(hist[t], &bins[((size_t)t * G + g) * 16]);
        STAMP(0,6);
    } else if (b < G + nNodeBlocks) {
        const int nb = b - G;
        const int n = lane & 15, q = lane >> 4;
        const int node = nb * NODES_PB + w * 16 + n;
        const int nodec = min(node, N - 1);
        u32x4 sb[4];
#pragma unroll
        for (int ks = 0; ks < 4; ++ks) sb[ks] = *(const u32x4*)(sv + (size_t)nodec * 512 + ks * 32 + q * 8);
#pragma unroll
        for (int i = 0; i < 2; ++i) {
            const int u = t + i * PRE_T, k = u >> 4, cu = u & 15;
            u32x4 v = *(const u32x4*)(w1 + (size_t)u * 8);
            if ((k >> 3) & 1) { u32x4 x; x[0] = v[2]; x[1] = v[3]; x[2] = v[0]; x[3] = v[1]; v = x; }
            *(u32x4*)(smem + k * 256 + ((cu * 16) ^ ((k & 3) << 6))) = v;
        }
        STAMP(0,1);
        __syncthreads();
        STAMP(0,2);
        const int r = (lane & 15) >> 2, cc = lane & 3;
#pragma unroll
        for (int tt = 0; tt < 4; ++tt) {
            f32x4 acc[2];
#pragma unroll
            for (int hf = 0; hf < 2; ++hf) {
                acc[hf] = (f32x4){0.f, 0.f, 0.f, 0.f};
#pragma unroll
                for (int ks = 0; ks < 4; ++ks) {
                    const int col = (64 * tt + 16 * cc + 8 * hf) ^ (r << 6) ^ ((q & 1) << 3);
                    const int k0 = 32 * ks + 8 * q + r;
                    const u32x2 a0 = tr_read(smem + k0 * 256 + col);
                    const u32x2 a1 = tr_read(smem + (k0 + 4) * 256 + col);
                    u32x4 a; a[0] = a0[0]; a[1] = a0[1]; a[2] = a1[0]; a[3] = a1[1];
                    acc[hf] = mfma16(a, sb[ks], acc[hf]);
                }
            }
            u32x4 o;
#pragma unroll
            for (int hf = 0; hf < 2; ++hf) {
                float y[4];
#pragma unroll
                for (int i = 0; i < 4; ++i) {
                    const float x = rbf16(acc[hf][i]);
                    y[i] = x * __builtin_amdgcn_rcpf(1.0f + __expf(-x));
                }
                o[2 * hf] = pack2(y[0], y[1]); o[2 * hf + 1] = pack2(y[2], y[3]);
            }
            if (node < N) hstore16(o, h + (size_t)node * 128 + 32 * tt + 8 * q);
            STAMP(0,3+tt);
        }
    } else {
        const int T = (b - G - nNodeBlocks) * PRE_T + t;
        const int fi = T >> 6;
        const int ww = fi / 12, gg = (fi % 12) >> 2, ks = fi & 3;
        const int c = lane & 15, q = lane >> 4;
        u32 v[8];
#pragma unroll
        for (int j = 0; j < 8; ++j) v[j] = w2[(size_t)(32 * ks + 8 * q + j) * 384 + 128 * gg + 16 * ww + c];
        u32x4 o;
#pragma unroll
        for (int j = 0; j < 4; ++j) o[j] = v[2 * j] | (v[2 * j + 1] << 16);
        w2f[T] = o;
        if (ks == 0) {
            u32 r[4];
#pragma unroll
            for (int k = 0; k < 4; ++k) r[k] = wrbf[k * 384 + 128 * gg + 16 * ww + c];
            u32x2 p; p[0] = q < 2 ? (r[0] | (r[1] << 16)) : 0u; p[1] = q < 2 ? (r[2] | (r[3] << 16)) : 0u;
            wrf[(ww * 3 + gg) * 64 + lane] = p;
        }
    }
}

#define RING_POS 3
#define RES_POS 1
#define UNPACK_MFMA 0
#define RING_LOAD0(base, o0, o1)                                                                             \
    asm volatile("global_load_dwordx4 v[240:243], %0, %2\n\tglobal_load_dwordx4 v[244:247], %1, %2" ::"v"(o0), "v"(o1), "s"(base) \
                 : "memory", "v240", "v241", "v242", "v243", "v244", "v245", "v246", "v247")
#define RING_LOAD1(base, o0, o1)                                                                             \
    asm volatile("global_load_dwordx4 v[248:251], %0, %2\n\tglobal_load_dwordx4 v[252:255], %1, %2" ::"v"(o0), "v"(o1), "s"(base) \
                 : "memory", "v248", "v249", "v250", "v251", "v252", "v253", "v254", "v255")
#define RING_WRITE0(la) asm volatile("ds_write_b128 %0, v[240:243]\n\tds_write_b128 %0, v[244:247] offset:1040" ::"v"(la) : "memory")
#define RING_WRITE1(la) asm volatile("ds_write_b128 %0, v[248:251]\n\tds_write_b128 %0, v[252:255] offset:1040" ::"v"(la) : "memory")
DI void dma16c(const void* gsrc, u32 lds_base, int rem) {
    asm volatile("s_cmp_lt_i32 %2, 1\n\ts_cbranch_scc1 1f\n\ts_mov_b32 m0, %1\n\ts_nop 0\n\tglobal_load_lds_dwordx4 %0, off\n1:"
                 ::"v"(gsrc), "s"(lds_base), "s"(rem) : "memory", "scc");
}
#define SB() __builtin_amdgcn_sched_barrier(0)
DI void astore16nt(u32x4 d, void* base, u32 off) {
    asm volatile("global_store_dwordx4 %0, %1, %2 nt\n\ts_nop 1" ::"v"(off), "v"(d), "s"(base) : "memory");
}
DI u32x4 onehot(u32x2 dA, u32x2 dB, u32 tg, u32 kOne) {
    u32x4 S;
    const u32 kNeg = 0x80008000u;
    asm volatile(
        "v_xor_b32 %0, %4, %8\n\tv_xor_b32 %1, %5, %8\n\tv_xor_b32 %2, %6, %8\n\tv_xor_b32 %3, %7, %8\n\t"
        "v_pk_mad_i16 %0, %0, %9, %10 clamp\n\tv_pk_mad_i16 %1, %1, %9, %10 clamp\n\t"
        "v_pk_mad_i16 %2, %2, %9, %10 clamp\n\tv_pk_mad_i16 %3, %3, %9, %10 clamp\n\ts_nop 1"
        : "=&v"(S[0]), "=&v"(S[1]), "=&v"(S[2]), "=&v"(S[3])
        : "v"(dA[0]), "v"(dA[1]), "v"(dB[0]), "v"(dB[1]), "v"(tg), "s"(kNeg), "v"(kOne));
    return S;
}

__global__ __launch_bounds__(512, 2) __attribute__((amdgpu_num_vgpr(240))) void main_kernel(const u16* __restrict__ sv, const u16* __restrict__ pos,
                                                      const int* __restrict__ src, const int* __restrict__ dst,
                                                      const u16* __restrict__ freqs, const u16* __restrict__ h,
                                                      const u32x4* __restrict__ w2f, const u32x2* __restrict__ wrf,
                                                      const u32* __restrict__ bins, u16* __restrict__ out, int N, int E,
                                                      int NT) {
    extern __shared__ __attribute__((aligned(16))) char smem[];
    const int t = threadIdx.x, lane = t & 63;
    const int w = __builtin_amdgcn_readfirstlane(t >> 6);
    const int b = blockIdx.x;
    const int tile = (b & 7) * (TILES / 8) + (b >> 3);
    const int c16 = lane & 15, q = lane >> 4;
    const int node0 = tile * NT;
    const int nvalid = min(NT, N - node0);
    if (nvalid <= 0) return;
    const u32 lbase = (u32)(size_t)(lds_char*)smem;
    MST_DECL;
    MST(0);

    u32x2 fq;
    aload8(fq, freqs, 0u);
    u32x4 bw;
    aload16(bw, (const char*)bins + (size_t)tile * (G * 64), (u32)t * 16u);
    u32 pdv;
    aload2(pdv, pos + (size_t)node0 * 3, (u32)min(t, 3 * nvalid - 1) * 2u);
    u32x4 W2[3][4];
#pragma unroll
    for (int gg = 0; gg < 3; ++gg)
#pragma unroll
        for (int ks = 0; ks < 4; ++ks) aload16(W2[gg][ks], w2f, (u32)((w * 12 + gg * 4 + ks) * 64 + lane) * 16u);
    u32x2 WR[3];
#pragma unroll
    for (int gg = 0; gg < 3; ++gg) aload8(WR[gg], wrf, (u32)((w * 3 + gg) * 64 + lane) * 8u);
    const char* const rbase = (const char*)sv + (size_t)node0 * 1024;
#define RES_OFF(rb, i) ((u32)min((rb) * 16 + 2 * w + (i), nvalid - 1) * 1024u + (u32)lane * 16u)
#define RES_FIRST()                                                                                         \
    do {                                                                                                    \
        _Pragma("unroll") for (int i = 0; i < 4; ++i)                                                       \
            dma16c(rbase + RES_OFF(i >> 1, i & 1), lbase + L_STG + (u32)((i >> 1) * STAGE_B + (2 * w + (i & 1)) * SPITCH), 1); \
    } while (0)
#define RING_EARLY()                                                                                        \
    do {                                                                                                    \
        RING_LOAD0(rbase, RES_OFF(2, 0), RES_OFF(2, 1));                                                    \
        RING_LOAD1(rbase, RES_OFF(3, 0), RES_OFF(3, 1));                                                    \
    } while (0)
#define RING_FIRST()                                                                                        \
    do {                                                                                                    \
        RING_LOAD0(rbase, RES_OFF(2, 0), RES_OFF(2, 1));                                                    \
        issued += 2; mR0 = issued;                                                                          \
        RING_LOAD1(rbase, RES_OFF(3, 0), RES_OFF(3, 1));                                                    \
        issued += 2; mR1 = issued;                                                                          \
    } while (0)

    u32* const l_tmp = (u32*)(smem + L_BUF + 2 * CHUNK_B);
    u32* const l_fin = (u32*)(smem + L_FIN);
    u32* const l_hist = (u32*)(smem + L_HIST);
    u32* const l_start = (u32*)(smem + L_START);
    float* const l_posd = (float*)(smem + L_POSD);
    u32* const l_misc = (u32*)(smem + L_MISC);
    u16* const l_dl = (u16*)(smem + L_DL);
    float* const l_rh = (float*)(smem + L_RH);

    if (t < 128) l_hist[t] = 0;
    if (t == 128) l_misc[0] = 0;
    lds_barrier();

    VMWAIT(15);
    asm volatile("" : "+v"(bw), "+v"(pdv), "+v"(fq));
    const float fr0 = __uint_as_float(fq[0] << 16), fr1 = __uint_as_float(fq[0] & 0xffff0000u);
    const float fr2 = __uint_as_float(fq[1] << 16), fr3 = __uint_as_float(fq[1] & 0xffff0000u);
    MST(1);
    if (t < 3 * nvalid) l_posd[t] = bf2f(pdv);
    const u32 cnt = (u32)__builtin_amdgcn_update_dpp(0, (int)bw[0], 0x00, 0xf, 0xf, false);
    if (cnt > 15u) l_misc[0] = 1;
    u32 rk[4];
    bool vl[4];
#pragma unroll
    for (int i = 0; i < 4; ++i) {
        const u32 widx = 4 * (t & 3) + i;
        vl[i] = widx >= 1 && widx <= cnt && cnt <= 15u;
        rk[i] = 0;
        if (vl[i]) rk[i] = atomicAdd(&l_hist[bw[i] >> 24], 1u);
    }
    RES_FIRST();
    lds_barrier();
    u32 starts;
    int total;
    {
        const u32x2 hh = *(const u32x2*)(l_hist + 2 * lane);
        const u32 inc = wave_incl_scan(hh[0] + hh[1]);
        const u32 s0 = inc - hh[0] - hh[1], s1 = s0 + hh[0];
        starts = s0 | (s1 << 16);
        total = __builtin_amdgcn_readlane((int)inc, 63);
        if (w == 0) { u32x2 sx; sx[0] = s0; sx[1] = s1; *(u32x2*)(l_start + 2 * lane) = sx; if (lane == 63) l_start[128] = inc; }
    }
    const bool slow = __builtin_amdgcn_readfirstlane((int)l_misc[0]) != 0;
#pragma unroll
    for (int i = 0; i < 4; ++i) {
        const u32 loc = (bw[i] >> 24) & 127u;
        const u32 p = (u32)__builtin_amdgcn_ds_bpermute((int)((loc >> 1) << 2), (int)starts);
        const u32 st = (loc & 1) ? (p >> 16) : (p & 0xffffu);
        if (vl[i]) l_tmp[st + rk[i]] = bw[i];
    }
    if (t >= 480) l_fin[total + t - 480] = 0;
    lds_barrier();
    const u32 posLast = (u32)max(6 * N - 8, 0);
#define POS_OFF(sidx) min((sidx) * 6u, posLast)
    const bool early = !slow && total <= CAP;
    u32 erec = l_tmp[max(min(t, total - 1), 0)];
    if (total == 0) erec = 0;
    u32x2 epos;
    aload8(epos, pos, POS_OFF(erec & 0xffffffu));
    int egp = 0;
    for (int p = t; p < total; p += 512) {
        const u32 rec = l_tmp[p];
        const u32 loc = rec >> 24;
        const int s = (int)l_start[loc], e = (int)l_start[loc + 1];
        int rank = 0;
        for (int j = s; j < e; j += 2) {
            const u32 k0 = l_tmp[j], k1 = l_tmp[j + 1];
            rank += (k0 < rec || (k0 == rec && j < p)) ? 1 : 0;
            rank += (j + 1 < e && (k1 < rec || (k1 == rec && j + 1 < p))) ? 1 : 0;
        }
        l_fin[s + rank] = rec;
        egp = s + rank;
    }
    lds_barrier();
    MST(2);

    f32x4 acc[4];
#pragma unroll
    for (int x = 0; x < 4; ++x) acc[x] = (f32x4){0.f, 0.f, 0.f, 0.f};
    int cur = 0;
    int sto = 0;
    int epoch = 0, eS0 = -1, eS1 = -1;
    int issued = 0;
    int mR0 = 0, mR1 = 0;

    const u32 dshift = lane < 16 ? 8u : 10u;
    const char* const dbase = (lane < 16 ? (const char*)h : (const char*)sv) + lane * 16;
    char* const obase = (char*)out + (size_t)node0 * 1024;
    const u32 kOne = 0x3F803F80u;
    u32x2 ident;
    ident[0] = (4 * q == c16 ? 0x3F80u : 0u) | (4 * q + 1 == c16 ? 0x3F800000u : 0u);
    ident[1] = (4 * q + 2 == c16 ? 0x3F80u : 0u) | (4 * q + 3 == c16 ? 0x3F800000u : 0u);
    u32 tg = (u32)(2 * c16) * 0x10001u;

#define STORE_ONE(d0_, d1_)                                                                                 \
    do {                                                                                                    \
        const u32 la_ = lbase + L_STG + (u32)((sto & 1) * STAGE_B + 2 * w * SPITCH) + (u32)lane * 16u;      \
        asm volatile("" : "+v"(d0_), "+v"(d1_));                                                            \
        if (sto < 5) {                                                                                      \
            if (sto & 1) {                                                                                  \
                wait_vm(issued - mR1);                                                                      \
                RING_WRITE1(la_);                                                                           \
                if (sto < 3) {                                                                              \
                    RING_LOAD1(rbase, RES_OFF(sto + 4, 0), RES_OFF(sto + 4, 1));                            \
                    issued += 2; mR1 = issued;                                                              \
                }                                                                                           \
                eS1 = epoch;                                                                                \
            } else {                                                                                        \
                wait_vm(issued - mR0);                                                                      \
                RING_WRITE0(la_);                                                                           \
                if (sto < 3) {                                                                              \
                    RING_LOAD0(rbase, RES_OFF(sto + 4, 0), RES_OFF(sto + 4, 1));                            \
                    issued += 2; mR0 = issued;                                                              \
                }                                                                                           \
                eS0 = epoch;                                                                                \
            }                                                                                               \
        }                                                                                                   \
        const int row_ = sto * 16 + 2 * w;                                                                  \
        const u32 oo_ = (u32)row_ * 1024u + (u32)lane * 16u;                                                \
        if (row_ < nvalid) { astore16nt(d0_, obase, oo_); ++issued; }                                       \
        if (row_ + 1 < nvalid) { astore16nt(d1_, obase, oo_ + 1024u); ++issued; }                           \
        ++sto;                                                                                              \
    } while (0)
#define STG_ROWS(rb) (smem + L_STG + ((rb) & 1) * STAGE_B + 2 * w * SPITCH + lane * 16)
#define EPOCH_BARRIER()                                                                                     \
    do {                                                                                                    \
        const int lim_ = cur;                                                                               \
        lds_barrier();                                                                                      \
        ++epoch;                                                                                            \
        while (sto < lim_) {                                                                                \
            u32x4 e0_ = *(const u32x4*)STG_ROWS(sto), e1_ = *(const u32x4*)(STG_ROWS(sto) + SPITCH);        \
            STORE_ONE(e0_, e1_);                                                                            \
        }                                                                                                   \
    } while (0)
#define READY() (sto >= cur - 1 && epoch > ((cur & 1) ? eS1 : eS0))
#define RES_READ(rt)                                                                                        \
    _Pragma("unroll") for (int x_ = 0; x_ < 4; ++x_)                                                        \
        rt[x_] = tr_read(smem + L_STG + (cur & 1) * STAGE_B + 32 * w + (4 * q + (c16 >> 2)) * SPITCH + 8 * (c16 & 3) + 256 * x_)
#define FLUSH_CUR(rt, have)                                                                                 \
    do {                                                                                                    \
        if (!(have)) {                                                                                      \
            while (!READY()) EPOCH_BARRIER();                                                               \
            RES_READ(rt);                                                                                   \
        }                                                                                                   \
        char* sb_ = smem + L_STG + (cur & 1) * STAGE_B + 32 * w;                                            \
        _Pragma("unroll") for (int x_ = 0; x_ < 4; ++x_) {                                                  \
            const f32x4 f_ = mfma16k16(rt[x_], ident, acc[x_]);                                             \
            u32x2 o_; o_[0] = pack2(f_[0], f_[1]); o_[1] = pack2(f_[2], f_[3]);                             \
            *(u32x2*)(sb_ + c16 * SPITCH + 8 * q + 256 * x_) = o_;                                          \
        }                                                                                                   \
        ++cur; tg += 32u * 0x10001u;                                                                        \
    } while (0)
#define SEG(rt, have)                                                                                       \
    do {                                                                                                    \
        int endc_ = __builtin_amdgcn_readlane((int)rbtab, cur);                                             \
        if (endc_ > sEb) {                                                                                  \
            const u32x4 S_ = onehot(dA, dB, tg, kOne);                                                      \
            _Pragma("unroll") for (int x_ = 0; x_ < 4; ++x_) acc[x_] = mfma16(A[x_], S_, acc[x_]);          \
        }                                                                                                   \
        bool have_ = (have);                                                                                \
        while (endc_ < sE1) {                                                                               \
            FLUSH_CUR(rt, have_);                                                                           \
            have_ = false;                                                                                  \
            endc_ = __builtin_amdgcn_readlane((int)rbtab, cur);                                             \
            const u32x4 S_ = onehot(dA, dB, tg, kOne);                                                      \
            _Pragma("unroll") for (int x_ = 0; x_ < 4; ++x_)                                                \
                acc[x_] = mfma16(A[x_], S_, (f32x4){0.f, 0.f, 0.f, 0.f});                                   \
        }                                                                                                   \
    } while (0)

    int W0 = 0;
    int slowR = 0, slowE = 0;
    bool first = true;
    for (;;) {
        int nrec;
        u32 rbtab;
        const u32* list;
        if (!slow) {
            if (W0 >= total) break;
            nrec = min(CAP, total - W0);
            list = l_fin + W0;
            const u32 nx = (u32)__builtin_amdgcn_ds_bpermute(((lane & 7) + 1) * 32, (int)starts) & 0xffffu;
            rbtab = (u32)min(max((int)nx - W0, 0), nrec);
            W0 += CAP;
            if (!first) lds_barrier();
        } else {
            int n = 0;
            lds_barrier();
            while (slowR < 7) {
                if (slowE >= E) {
                    ++slowR; slowE = 0;
                    if (n > 0) { --slowR; slowE = E; break; }
                    continue;
                }
                const int e = slowE + t;
                int di = -1, si = 0;
                if (e < E) { di = dst[e]; si = src[e]; }
                asm volatile("" : "+v"(di), "+v"(si));
                const int loc = di - node0;
                const bool m = e < E && loc >= 0 && loc < nvalid && (loc >> 4) == slowR;
                const u64 bal = __ballot(m);
                const int pw = __builtin_amdgcn_mbcnt_hi((u32)(bal >> 32), __builtin_amdgcn_mbcnt_lo((u32)bal, 0));
                if (lane == 0) l_hist[w] = (u32)__popcll(bal);
                lds_barrier();
                int basep = 0, all = 0;
#pragma unroll
                for (int i = 0; i < 8; ++i) { const int c = (int)l_hist[i]; basep += i < w ? c : 0; all += c; }
                all = __builtin_amdgcn_readfirstlane(all);
                const bool fits = n + all <= CAP;
                if (fits && m) l_fin[n + basep + pw] = ((u32)loc << 24) | (u32)si;
                lds_barrier();
                if (!fits) break;
                n += all; slowE += 512;
            }
            if (n == 0) {
                if (slowR >= 7) break;
            }
            if (t < 32) l_fin[n + t] = 0;
            lds_barrier();
            nrec = n;
            list = l_fin;
            rbtab = lane < slowR ? 0u : (u32)n;
            if (slowE >= E) { ++slowR; slowE = 0; }
            if (n == 0) continue;
        }
        nrec = __builtin_amdgcn_readfirstlane(nrec);
        const int nch = (nrec + 31) >> 5;
        int mA, mB, mC;
        u32 bA = lbase + L_BUF, bB = lbase + L_BUF + CHUNK_B, bC = lbase + L_BUF + 2 * CHUNK_B;
        const u32* const listw = list + 4 * w;
#define DMA_ROW(r4, i_, buf, en) dma16c(dbase + ((r4)[i_] << dshift), (buf) + (u32)(4 * w + (i_)) * PITCH, en)
        u32x4 nr4;
#define DMA_PROLOGUE()                                                                                      \
    do {                                                                                                    \
        const u32x4 r0 = *(const u32x4*)listw, r1 = *(const u32x4*)(listw + 32);                            \
        nr4 = *(const u32x4*)(listw + 64);                                                                  \
        _Pragma("unroll") for (int i = 0; i < 4; ++i) DMA_ROW(r0, i, bA, 1);                                \
        issued += 4;                                                                                        \
        mA = issued;                                                                                        \
        const int rem1 = nch - 1, en1 = rem1 > 0 ? 1 : 0;                                                   \
        _Pragma("unroll") for (int i = 0; i < 4; ++i) DMA_ROW(r1, i, bB, rem1);                             \
        issued += 4 * en1;                                                                                  \
        mB = issued;                                                                                        \
        if (first) RING_FIRST();                                                                            \
        mC = issued;                                                                                        \
    } while (0)
#define GEOMETRY(rec, pp, gp)                                                                               \
    do {                                                                                                    \
        const u32 loc = (rec) >> 24, sidx = (rec) & 0xffffffu;                                              \
        const bool shifted = sidx * 6u > posLast;                                                           \
        const u32 pxy = shifted ? __builtin_amdgcn_alignbit((pp)[1], (pp)[0], 16) : (pp)[0];                \
        const u32 pzz = shifted ? (pp)[1] >> 16 : (pp)[1];                                                  \
        const float rx = rbf16(lo16(pxy) - l_posd[3 * loc]);                                                \
        const float ry = rbf16(hi16(pxy) - l_posd[3 * loc + 1]);                                            \
        const float rz = rbf16(lo16(pzz) - l_posd[3 * loc + 2]);                                            \
        const float ss = rbf16(rbf16(rx * rx) + rbf16(ry * ry) + rbf16(rz * rz));                           \
        const float d = rbf16(sqrtf(rbf16(ss + 1e-8f)));                                                    \
        const float inv = __builtin_amdgcn_rcpf(d);                                                         \
        const float rh0 = rbf16(rx * inv), rh1 = rbf16(ry * inv), rh2 = rbf16(rz * inv);                    \
        const float i2pi = 0.15915494309189535f;                                                            \
        float ang = rbf16(rbf16(3.14159265358979323846f * d) / 5.0f);                                       \
        float fc = rbf16(__builtin_amdgcn_cosf(__builtin_amdgcn_fractf(ang * i2pi)));                       \
        fc = rbf16(0.5f * rbf16(fc + 1.0f));                                                                \
        fc = d < 5.0f ? fc : 0.0f * d;                                                                      \
        float cf[4];                                                                                        \
        const float fr[4] = {fr0, fr1, fr2, fr3};                                                           \
        _Pragma("unroll") for (int k = 0; k < 4; ++k) {                                                     \
            const float arg = rbf16(d * fr[k]);                                                             \
            const float sn = rbf16(__builtin_amdgcn_sinf(__builtin_amdgcn_fractf(arg * i2pi)));             \
            cf[k] = rbf16(sn * inv) * fc;                                                                   \
        }                                                                                                   \
        float hi[4], lo[4];                                                                                 \
        _Pragma("unroll") for (int k = 0; k < 4; ++k) { hi[k] = rbf16(cf[k]); lo[k] = cf[k] - hi[k]; }      \
        u32x4 a;                                                                                            \
        a[0] = pack2(hi[0], hi[1]); a[1] = pack2(hi[2], hi[3]); a[2] = pack2(lo[0], lo[1]); a[3] = pack2(lo[2], lo[3]); \
        *(u32x4*)(smem + L_WFA + (gp) * 16) = a;                                                            \
        l_rh[gp] = rh0; l_rh[CAP + (gp)] = rh1; l_rh[2 * CAP + (gp)] = rh2;                                 \
        l_dl[gp] = (u16)(2 * loc);                                                                          \
    } while (0)
        if (early) {
            DMA_PROLOGUE();
            MST(3);
            wait_vm(issued);
            asm volatile("" : "+v"(epos));
            MST(4);
            if (t < nrec) GEOMETRY(erec, epos, egp);
        } else {
            const u32 rec = list[min(t, nrec - 1)];
            u32x2 pp;
            aload8(pp, pos, POS_OFF(rec & 0xffffffu));
            issued += 1;
            const int mPos = issued;
            DMA_PROLOGUE();
            wait_vm(issued - mPos);
            asm volatile("" : "+v"(pp));
            if (t < nrec) GEOMETRY(rec, pp, t);
        }
        if (t >= nrec && t < nch * 32) {
            *(u32x4*)(smem + L_WFA + t * 16) = (u32x4){0u, 0u, 0u, 0u};
            l_rh[t] = 0.f; l_rh[CAP + t] = 0.f; l_rh[2 * CAP + t] = 0.f;
            l_dl[t] = (u16)0x7FFEu;
        }
        if (first) {
#pragma unroll
            for (int gg = 0; gg < 3; ++gg) {
                asm volatile("" : "+v"(WR[gg]));
#pragma unroll
                for (int ks = 0; ks < 4; ++ks) asm volatile("" : "+v"(W2[gg][ks]));
            }
            first = false;
        }
        MST(5);

        u32x4 A[4];
        u32x2 dA, dB;
        int sEb = 0, sE1 = 0;
#pragma unroll
        for (int x = 0; x < 4; ++x) A[x] = (u32x4){0u, 0u, 0u, 0u};
        dA = dB = (u32x2){0u, 0u};
        for (int ch = 0; ch < nch; ++ch) {
            if (ch < 10) MST(8+5*ch);
            wait_vm(issued - mA);
            if (ch < 10) MSTI(9+5*ch);
            lds_barrier();
            ++epoch;
            if (ch < 10) MSTI(10+5*ch);
            const char* B = smem + (bA - lbase);
            const int eb = ch * 32;
            const int stoTop = sto, curTop = cur;
            const bool pref = READY();
            const int rem = nch - ch - 2, en = rem > 0 ? 1 : 0;
            u32x2 wa[2], rt[4];
            u32x4 a0[4], a1[4];
#pragma unroll
            for (int blk = 0; blk < 2; ++blk) wa[blk] = *(const u32x2*)(smem + L_WFA + (eb + 16 * blk + c16) * 16 + (q & 1) * 8);
            RES_READ(rt);
#pragma unroll
            for (int ks = 0; ks < 4; ++ks) a0[ks] = *(const u32x4*)(B + c16 * PITCH + ks * 64 + q * 16);
            u32x4 sd0 = *(const u32x4*)STG_ROWS(sto), sd1 = *(const u32x4*)(STG_ROWS(sto) + SPITCH);
#pragma unroll
            for (int ks = 0; ks < 4; ++ks) a1[ks] = *(const u32x4*)(B + (16 + c16) * PITCH + ks * 64 + q * 16);
            SB();
            if (ch >= 1) SEG(rt, pref);
            if (ch < 10) MSTI(11+5*ch);
            SB();
            u32x2 vv[3];
            f32x4 rh[3];
#define V_READ(blk)                                                                                         \
    _Pragma("unroll") for (int x = 0; x < 3; ++x) {                                                         \
        vv[x] = tr_read(B + (16 * (blk) + 4 * q + (c16 >> 2)) * PITCH + 256 * (1 + x) + 32 * w + 8 * (c16 & 3)); \
        rh[x] = *(const f32x4*)(l_rh + x * CAP + eb + 16 * (blk) + 4 * q);                                  \
    }
#define MSG_HEAD(blk)                                                                                       \
    do {                                                                                                    \
        const f32x4 ms = Z[0] * Wf[0];                                                                      \
        mvv = Z[1] * Wf[1]; mvr = Z[2] * Wf[2];                                                             \
        A[0][2 * (blk)] = pack2(ms[0], ms[1]); A[0][2 * (blk) + 1] = pack2(ms[2], ms[3]);                   \
    } while (0)
#define MSG_V(blk, x)                                                                                       \
    do {                                                                                                    \
        const f32x4 vf = VUNPACK(vv[x]);                                                                    \
        const f32x4 u = mvv * vf + mvr * rh[x];                                                             \
        A[1 + (x)][2 * (blk)] = pack2(u[0], u[1]); A[1 + (x)][2 * (blk) + 1] = pack2(u[2], u[3]);           \
    } while (0)
#define VUNPACK(v) ((f32x4){lo16((v)[0]), hi16((v)[0]), lo16((v)[1]), hi16((v)[1])})
            f32x4 Z[3], Wf[3], Y[3], Wg[3], mvv, mvr;
            V_READ(0);
#pragma unroll
            for (int gg = 0; gg < 3; ++gg) Z[gg] = mfma16(a0[0], W2[gg][0], (f32x4){0.f, 0.f, 0.f, 0.f});
#pragma unroll
            for (int gg = 0; gg < 3; ++gg) Z[gg] = mfma16(a0[1], W2[gg][1], Z[gg]);
            SB();
            DMA_ROW(nr4, 0, bC, rem);
            issued += en;
            SB();
#pragma unroll
            for (int gg = 0; gg < 3; ++gg) Wf[gg] = mfma16k16(wa[0], WR[gg], (f32x4){0.f, 0.f, 0.f, 0.f});
#pragma unroll
            for (int gg = 0; gg < 3; ++gg) Z[gg] = mfma16(a0[2], W2[gg][2], Z[gg]);
#pragma unroll
            for (int gg = 0; gg < 3; ++gg) Z[gg] = mfma16(a0[3], W2[gg][3], Z[gg]);
            SB();
            DMA_ROW(nr4, 1, bC, rem);
            issued += en;
            SB();
            if (sto == stoTop && stoTop < curTop) STORE_ONE(sd0, sd1);
            if (ch < 10) MSTI(12+5*ch);
            SB();
#pragma unroll
            for (int gg = 0; gg < 3; ++gg) Y[gg] = mfma16(a1[0], W2[gg][0], (f32x4){0.f, 0.f, 0.f, 0.f});
            MSG_HEAD(0);
            SB();
#pragma unroll
            for (int gg = 0; gg < 3; ++gg) Y[gg] = mfma16(a1[1], W2[gg][1], Y[gg]);
            MSG_V(0, 0);
            SB();
            DMA_ROW(nr4, 2, bC, rem);
            issued += en;
            SB();
#pragma unroll
            for (int gg = 0; gg < 3; ++gg) Y[gg] = mfma16(a1[2], W2[gg][2], Y[gg]);
            MSG_V(0, 1);
            SB();
#pragma unroll
            for (int gg = 0; gg < 3; ++gg) Y[gg] = mfma16(a1[3], W2[gg][3], Y[gg]);
            MSG_V(0, 2);
            SB();
            V_READ(1);
            dA = *(const u32x2*)(l_dl + eb + 4 * q); dB = *(const u32x2*)(l_dl + eb + 16 + 4 * q);
#pragma unroll
            for (int gg = 0; gg < 3; ++gg) Wg[gg] = mfma16k16(wa[1], WR[gg], (f32x4){0.f, 0.f, 0.f, 0.f});
            SB();
            DMA_ROW(nr4, 3, bC, rem);
            issued += en; mC = issued;
            nr4 = *(const u32x4*)(listw + (ch + 3) * 32);
            SB();
#pragma unroll
            for (int gg = 0; gg < 3; ++gg) { Z[gg] = Y[gg]; Wf[gg] = Wg[gg]; }
            MSG_HEAD(1);
            MSG_V(1, 0); MSG_V(1, 1); MSG_V(1, 2);
            sEb = eb; sE1 = min(eb + 32, nrec);
            mA = mB; mB = mC;
            const u32 tb = bA; bA = bB; bB = bC; bC = tb;
        }
        {
            u32x2 rt[4];
            SEG(rt, false);
        }
    }
    MST(6);
    if (first) {
        RING_FIRST();
        VMWAIT(0);
        lds_barrier();
        ++epoch;
    }
    while (cur < 7) {
        u32x2 rt[4];
        FLUSH_CUR(rt, false);
#pragma unroll
        for (int x = 0; x < 4; ++x) acc[x] = (f32x4){0.f, 0.f, 0.f, 0.f};
    }
    EPOCH_BARRIER();
    MST(7);
    MST_OUT();
}

extern "C" void kernel_launch(void* const* d_in, const int* in_sizes, int n_in, void* d_out, int out_size, void* d_ws,
                              size_t ws_size, hipStream_t stream) {
    const u16* sv = (const u16*)d_in[0];
    const u16* pos = (const u16*)d_in[1];
    const int* src = (const int*)d_in[2];
    const int* dst = (const int*)d_in[3];
    const u16* freqs = (const u16*)d_in[4];
    const u16* w1 = (const u16*)d_in[5];
    const u16* w2 = (const u16*)d_in[6];
    const u16* wrbf = (const u16*)d_in[7];
    u16* out = (u16*)d_out;
    const int N = in_sizes[0] / 512;
    const int E = in_sizes[2];
    const int NT = (N + TILES - 1) / TILES;
    const int EPB = ((E + G - 1) / G + 3) / 4 * 4;
    const u32 ntmagic = (u32)((0x100000000ull + NT - 1) / NT);
    const int nNodeBlocks = (N + NODES_PB - 1) / NODES_PB;

    char* ws = (char*)d_ws;
    size_t off = 0;
    u16* h = (u16*)(ws + off); off += ((size_t)N * 256 + 255) / 256 * 256;
    u32x4* w2f = (u32x4*)(ws + off); off += 6144 * 16;
    u32x2* wrf = (u32x2*)(ws + off); off += 24 * 64 * 8;
    u32* bins = (u32*)(ws + off);

    const int posBytes = ((N * 6 + 15) / 16) * 16;
    const int preLds = posBytes + TILES * 4 + 64;
    (void)hipFuncSetAttribute((const void*)pre_kernel, hipFuncAttributeMaxDynamicSharedMemorySize, preLds);
    (void)hipFuncSetAttribute((const void*)main_kernel, hipFuncAttributeMaxDynamicSharedMemorySize, MAIN_LDS);
    pre_kernel<<<G + nNodeBlocks + W2_BLOCKS, PRE_T, preLds, stream>>>(sv, pos, src, dst, w1, w2, wrbf, h, w2f, wrf, bins, N, E,
                                                                       EPB, NT, ntmagic, nNodeBlocks);
    main_kernel<<<TILES, 512, MAIN_LDS, stream>>>(sv, pos, src, dst, freqs, h, w2f, wrf, bins, out, N, E, NT);
}
```

```cpp
#include <hip/hip_runtime.h>
#include <stdint.h>

typedef unsigned short u16;
typedef unsigned int u32;
typedef unsigned long long u64;
typedef __attribute__((ext_vector_type(8))) __bf16 bf16x8;
typedef __attribute__((ext_vector_type(2))) __bf16 bf16x2;
typedef __attribute__((ext_vector_type(4))) float f32x4;
typedef __attribute__((ext_vector_type(4))) u32 u32x4;
typedef __attribute__((ext_vector_type(2))) u32 u32x2;
typedef __attribute__((ext_vector_type(4))) int i32x4;
typedef __attribute__((ext_vector_type(4))) short s16x4;
typedef __attribute__((ext_vector_type(2))) u16 u16x2;
typedef __attribute__((address_space(3))) char lds_char;

#define DI __device__ __forceinline__
#define STAMP(k,i)
#define STAMPW(k,i,wv)
#define MST(i)
#define MSTI(i)
#define MST_DECL
#define MST_OUT()

constexpr int TILES = 256;
constexpr int G = 128;
constexpr int PRE_T = 1024;
constexpr int NODES_PB = 256;
constexpr int W2_BLOCKS = 6;
constexpr int CAP = 512;
constexpr int RC = 2048;
constexpr int PITCH = 1056;
constexpr int CHUNK_B = 32 * PITCH;
constexpr int SPITCH = 1040;
constexpr int STAGE_B = 16 * SPITCH;

constexpr int L_BUF = 0;
constexpr int L_STG = 3 * CHUNK_B;
constexpr int L_WFA = L_STG + 2 * STAGE_B;
constexpr int L_RH = L_WFA + CAP * 16;
constexpr int L_DL = L_RH + 3 * CAP * 4;
constexpr int L_FIN = L_DL + CAP * 2;
constexpr int L_HIST = L_FIN + RC * 4;
constexpr int L_START = L_HIST + 512;
constexpr int L_POSD = L_START + 544;
constexpr int L_MISC = L_POSD + 1344;
constexpr int MAIN_LDS = L_MISC + 64;
static_assert(MAIN_LDS <= 163840, "lds");

DI float bf2f(u32 b) { return __uint_as_float(b << 16); }
DI float rbf16(float x) { return (float)(__bf16)x; }
DI u32 pack2(float a, float b) { bf16x2 v; v[0] = (__bf16)a; v[1] = (__bf16)b; return __builtin_bit_cast(u32, v); }
DI float lo16(u32 p) { return __uint_as_float(p << 16); }
DI float hi16(u32 p) { return __uint_as_float(p & 0xffff0000u); }

DI u32 wave_incl_scan(u32 v) {
    v += (u32)__builtin_amdgcn_update_dpp(0, (int)v, 0x111, 0xf, 0xf, false);
    v += (u32)__builtin_amdgcn_update_dpp(0, (int)v, 0x112, 0xf, 0xf, false);
    v += (u32)__builtin_amdgcn_update_dpp(0, (int)v, 0x114, 0xf, 0xf, false);
    v += (u32)__builtin_amdgcn_update_dpp(0, (int)v, 0x118, 0xf, 0xf, false);
    v += (u32)__builtin_amdgcn_update_dpp(0, (int)v, 0x142, 0xa, 0xf, false);
    v += (u32)__builtin_amdgcn_update_dpp(0, (int)v, 0x143, 0xc, 0xf, false);
    return v;
}

DI void dma16(const void* gsrc, u32 lds_base) {
    asm volatile("s_mov_b32 m0, %1\n\ts_nop 0\n\tglobal_load_lds_dwordx4 %0, off" ::"v"(gsrc), "s"(lds_base) : "memory");
}
DI void lds_barrier() {
    asm volatile("s_waitcnt lgkmcnt(0)" ::: "memory");
    __builtin_amdgcn_s_barrier();
    asm volatile("" ::: "memory");
}
#define VMWAIT(n) asm volatile("s_waitcnt vmcnt(" #n ")" ::: "memory")
DI void wait_vm(int n) {
    if (n >= 8) {
        if (n >= 12) { if (n >= 16) VMWAIT(16); else if (n >= 14) VMWAIT(14); else VMWAIT(12); }
        else { if (n >= 10) VMWAIT(10); else VMWAIT(8); }
    } else if (n >= 4) {
        if (n >= 6) VMWAIT(6); else VMWAIT(4);
    } else {
        if (n >= 2) VMWAIT(2); else VMWAIT(0);
    }
}
typedef __attribute__((ext_vector_type(4))) short v4i16_t;
DI u32x2 tr_read(const char* p) {
    v4i16_t r = __builtin_amdgcn_ds_read_tr16_b64_v4i16((__attribute__((address_space(3))) v4i16_t*)p);
    return __builtin_bit_cast(u32x2, r);
}
DI f32x4 mfma16(u32x4 a, u32x4 b, f32x4 c) {
    return __builtin_amdgcn_mfma_f32_16x16x32_bf16(__builtin_bit_cast(bf16x8, a), __builtin_bit_cast(bf16x8, b), c, 0, 0, 0);
}
DI f32x4 mfma16k16(u32x2 a, u32x2 b, f32x4 c) {
    return __builtin_amdgcn_mfma_f32_16x16x16bf16_1k(__builtin_bit_cast(s16x4, a), __builtin_bit_cast(s16x4, b), c, 0, 0, 0);
}
DI void aload16(u32x4& d, const void* base, u32 off) {
    asm volatile("global_load_dwordx4 %0, %1, %2" : "=v"(d) : "v"(off), "s"(base) : "memory");
}
DI void aload8(u32x2& d, const void* base, u32 off) {
    asm volatile("global_load_dwordx2 %0, %1, %2" : "=v"(d) : "v"(off), "s"(base) : "memory");
}
DI void aload2(u32& d, const void* base, u32 off) {
    asm volatile("global_load_ushort %0, %1, %2" : "=v"(d) : "v"(off), "s"(base) : "memory");
}

#define WT_H 3
#define WT_B 3
#define HFLAGS " sc1"
#define BFLAGS " sc1"
DI void hstore16(u32x4 d, void* p) { asm volatile("global_store_dwordx4 %0, %1, off" HFLAGS "\n\ts_nop 1" ::"v"(p), "v"(d) : "memory"); }
#define WT_W 1
DI void hstore8(u32x2 d, void* p) { asm volatile("global_store_dwordx2 %0, %1, off" HFLAGS "\n\ts_nop 1" ::"v"(p), "v"(d) : "memory"); }
DI void bstore16(u32x4 d, void* p) { asm volatile("global_store_dwordx4 %0, %1, off" BFLAGS "\n\ts_nop 1" ::"v"(p), "v"(d) : "memory"); }
DI void bstore4(u32 d, void* p) { asm volatile("global_store_dword %0, %1, off" BFLAGS "\n\ts_nop 1" ::"v"(p), "v"(d) : "memory"); }

__global__ __launch_bounds__(PRE_T) void pre_kernel(const u16* __restrict__ sv, const u16* __restrict__ pos,
                                                    const int* __restrict__ src, const int* __restrict__ dst,
                                                    const u16* __restrict__ w1, const u16* __restrict__ w2,
                                                    const u16* __restrict__ wrbf, u16* __restrict__ h,
                                                    u32x4* __restrict__ w2f, u32x2* __restrict__ wrf, u32* __restrict__ bins,
                                                    int N, int E, int EPB, int NT, u32 ntmagic, int nNodeBlocks) {
    extern __shared__ __attribute__((aligned(16))) char smem[];
    const int t = threadIdx.x, lane = t & 63;
    const int w = __builtin_amdgcn_readfirstlane(t >> 6);
    const int b = blockIdx.x;
    STAMP(0,0);
    if (b < G) {
        const int g = (b & 7) * (G / 8) + (b >> 3);
        const int posUnits = (N * 6 + 15) >> 4;
        const int posBytes = posUnits * 16;
        u32* hist = (u32*)(smem + posBytes);
        const int imgOff = posBytes >= 16384 ? 0 : posBytes + 1024;
        const int e0 = g * EPB + t * 4;
        const int eEnd = min(E, (g + 1) * EPB);
        const int e0c = min(e0, E - 4);
        u32x4 s4, d4;
        aload16(s4, src, (u32)e0c * 4u);
        aload16(d4, dst, (u32)e0c * 4u);
        const u32 lbase = (u32)(size_t)(lds_char*)smem;
        for (int u0 = w * 64; u0 < posUnits; u0 += PRE_T) {
            const int u = u0 + lane;
            if ((u + 1) * 16 <= N * 6) dma16((const char*)pos + (size_t)u * 16, lbase + (u32)u0 * 16u);
        }
        if (t < TILES) hist[t] = 0;
        {
            const int tail0 = (N * 6) & ~15;
            if (t < ((N * 6 - tail0) >> 1)) ((u16*)(smem + tail0))[t] = pos[(tail0 >> 1) + t];
        }
        STAMP(0,1);
        VMWAIT(0);
        STAMP(0,2);
        asm volatile("" : "+v"(s4), "+v"(d4));
        lds_barrier();
        STAMP(0,3);
        const u32* posw = (const u32*)smem;
        u32 rec[4], slot[4];
#pragma unroll
        for (int i = 0; i < 4; ++i) {
            slot[i] = 0xFFFFFFFFu; rec[i] = 0;
            const bool ok = e0c + i >= e0 && e0c + i < eEnd;
            const u32 si = s4[i], di = d4[i];
            const u32 as = (3 * si) >> 1, ad = (3 * di) >> 1;
            const u32 as0 = posw[as], as1 = posw[as + 1];
            const u32 ad0 = posw[ad], ad1 = posw[ad + 1];
            const u32 shs = (si & 1) * 16, shd = (di & 1) * 16;
            const u32 sxy = __builtin_amdgcn_alignbit(as1, as0, shs), dxy = __builtin_amdgcn_alignbit(ad1, ad0, shd);
            const float rx = lo16(sxy) - lo16(dxy);
            const float ry = hi16(sxy) - hi16(dxy);
            const float rz = lo16(as1 >> shs) - lo16(ad1 >> shd);
            const float ss = rx * rx + ry * ry + rz * rz;
            const bool drop = (__float_as_uint(ss) - 0x41CA0000u) <= (0x7E000000u - 0x41CA0000u);
            if (ok && !drop) {
                const u32 tile = __umulhi(di, ntmagic);
                const u32 loc = di - tile * (u32)NT;
                const u32 rank = atomicAdd(&hist[tile], 1u);
                if (rank < 15u) { slot[i] = tile * 16u + 1u + rank; rec[i] = (loc << 24) | si; }
            }
        }
        STAMP(0,4);
        lds_barrier();
        u32* img = (u32*)(smem + imgOff);
#pragma unroll
        for (int i = 0; i < 4; ++i)
            if (slot[i] != 0xFFFFFFFFu) img[slot[i]] = rec[i];
        if (t < TILES) img[t * 16] = hist[t];
        lds_barrier();
        STAMP(0,5);
        {
            const u32x4 v = *(const u32x4*)(smem + imgOff + t * 16);
            bstore16(v, (char*)bins + ((size_t)(t >> 2) * G + g) * 64 + (t & 3) * 16);
        }
        STAMP(0,6);
    } else if (b < G + nNodeBlocks) {
        const int nb = b - G;
        const int n = lane & 15, q = lane >> 4;
        const int node = nb * NODES_PB + w * 16 + n;
        const int nodec = min(node, N - 1);
        u32x4 sb[4];
#pragma unroll
        for (int ks = 0; ks < 4; ++ks) sb[ks] = *(const u32x4*)(sv + (size_t)nodec * 512 + ks * 32 + q * 8);
#pragma unroll
        for (int i = 0; i < 2; ++i) {
            const int u = t + i * PRE_T, k = u >> 4, cu = u & 15;
            u32x4 v = *(const u32x4*)(w1 + (size_t)u * 8);
            if ((k >> 3) & 1) { u32x4 x; x[0] = v[2]; x[1] = v[3]; x[2] = v[0]; x[3] = v[1]; v = x; }
            *(u32x4*)(smem + k * 256 + ((cu * 16) ^ ((k & 3) << 6))) = v;
        }
        STAMP(0,1);
        __syncthreads();
        STAMP(0,2);
        const int r = (lane & 15) >> 2, cc = lane & 3;
#pragma unroll
        for (int tt = 0; tt < 4; ++tt) {
            f32x4 acc[2];
#pragma unroll
            for (int hf = 0; hf < 2; ++hf) {
                acc[hf] = (f32x4){0.f, 0.f, 0.f, 0.f};
#pragma unroll
                for (int ks = 0; ks < 4; ++ks) {
                    const int col = (64 * tt + 16 * cc + 8 * hf) ^ (r << 6) ^ ((q & 1) << 3);
                    const int k0 = 32 * ks + 8 * q + r;
                    const u32x2 a0 = tr_read(smem + k0 * 256 + col);
                    const u32x2 a1 = tr_read(smem + (k0 + 4) * 256 + col);
                    u32x4 a; a[0] = a0[0]; a[1] = a0[1]; a[2] = a1[0]; a[3] = a1[1];
                    acc[hf] = mfma16(a, sb[ks], acc[hf]);
                }
            }
            u32x4 o;
#pragma unroll
            for (int hf = 0; hf < 2; ++hf) {
                float y[4];
#pragma unroll
                for (int i = 0; i < 4; ++i) {
                    const float x = rbf16(acc[hf][i]);
                    y[i] = x * __builtin_amdgcn_rcpf(1.0f + __expf(-x));
                }
                o[2 * hf] = pack2(y[0], y[1]); o[2 * hf + 1] = pack2(y[2], y[3]);
            }
            if (node < N) hstore16(o, h + (size_t)node * 128 + 32 * tt + 8 * q);
            STAMP(0,3+tt);
        }
    } else {
        const int T = (b - G - nNodeBlocks) * PRE_T + t;
        const int fi = T >> 6;
        const int ww = fi / 12, gg = (fi % 12) >> 2, ks = fi & 3;
        const int c = lane & 15, q = lane >> 4;
        u32 v[8];
#pragma unroll
        for (int j = 0; j < 8; ++j) v[j] = w2[(size_t)(32 * ks + 8 * q + j) * 384 + 128 * gg + 16 * ww + c];
        u32x4 o;
#pragma unroll
        for (int j = 0; j < 4; ++j) o[j] = v[2 * j] | (v[2 * j + 1] << 16);
        hstore16(o, &w2f[T]);
        if (ks == 0) {
            u32 r[4];
#pragma unroll
            for (int k = 0; k < 4; ++k) r[k] = wrbf[k * 384 + 128 * gg + 16 * ww + c];
            u32x2 p; p[0] = q < 2 ? (r[0] | (r[1] << 16)) : 0u; p[1] = q < 2 ? (r[2] | (r[3] << 16)) : 0u;
            hstore8(p, &wrf[(ww * 3 + gg) * 64 + lane]);
        }
    }
}

#define RING_POS 3
#define RES_POS 1
#define UNPACK_MFMA 0
#define RING_LOAD0(base, o0, o1)                                                                             \
    asm volatile("global_load_dwordx4 v[240:243], %0, %2\n\tglobal_load_dwordx4 v[244:247], %1, %2" ::"v"(o0), "v"(o1), "s"(base) \
                 : "memory", "v240", "v241", "v242", "v243", "v244", "v245", "v246", "v247")
#define RING_LOAD1(base, o0, o1)                                                                             \
    asm volatile("global_load_dwordx4 v[248:251], %0, %2\n\tglobal_load_dwordx4 v[252:255], %1, %2" ::"v"(o0), "v"(o1), "s"(base) \
                 : "memory", "v248", "v249", "v250", "v251", "v252", "v253", "v254", "v255")
#define RING_WRITE0(la) asm volatile("ds_write_b128 %0, v[240:243]\n\tds_write_b128 %0, v[244:247] offset:1040" ::"v"(la) : "memory")
#define RING_WRITE1(la) asm volatile("ds_write_b128 %0, v[248:251]\n\tds_write_b128 %0, v[252:255] offset:1040" ::"v"(la) : "memory")
DI void dma16c(const void* gsrc, u32 lds_base, int rem) {
    asm volatile("s_cmp_lt_i32 %2, 1\n\ts_cbranch_scc1 1f\n\ts_mov_b32 m0, %1\n\ts_nop 0\n\tglobal_load_lds_dwordx4 %0, off\n1:"
                 ::"v"(gsrc), "s"(lds_base), "s"(rem) : "memory", "scc");
}
#define SB() __builtin_amdgcn_sched_barrier(0)
DI void astore16nt(u32x4 d, void* base, u32 off) {
    asm volatile("global_store_dwordx4 %0, %1, %2 sc1\n\ts_nop 1" ::"v"(off), "v"(d), "s"(base) : "memory");
}
DI u32x4 onehot(u32x2 dA, u32x2 dB, u32 tg, u32 kOne) {
    u32x4 S;
    const u32 kNeg = 0x80008000u;
    asm volatile(
        "v_xor_b32 %0, %4, %8\n\tv_xor_b32 %1, %5, %8\n\tv_xor_b32 %2, %6, %8\n\tv_xor_b32 %3, %7, %8\n\t"
        "v_pk_mad_i16 %0, %0, %9, %10 clamp\n\tv_pk_mad_i16 %1, %1, %9, %10 clamp\n\t"
        "v_pk_mad_i16 %2, %2, %9, %10 clamp\n\tv_pk_mad_i16 %3, %3, %9, %10 clamp\n\ts_nop 1"
        : "=&v"(S[0]), "=&v"(S[1]), "=&v"(S[2]), "=&v"(S[3])
        : "v"(dA[0]), "v"(dA[1]), "v"(dB[0]), "v"(dB[1]), "v"(tg), "s"(kNeg), "v"(kOne));
    return S;
}

__global__ __launch_bounds__(512, 2) __attribute__((amdgpu_num_vgpr(240))) void main_kernel(const u16* __restrict__ sv, const u16* __restrict__ pos,
                                                      const int* __restrict__ src, const int* __restrict__ dst,
                                                      const u16* __restrict__ freqs, const u16* __restrict__ h,
                                                      const u32x4* __restrict__ w2f, const u32x2* __restrict__ wrf,
                                                      const u32* __restrict__ bins, u16* __restrict__ out, int N, int E,
                                                      int NT) {
    extern __shared__ __attribute__((aligned(16))) char smem[];
    const int t = threadIdx.x, lane = t & 63;
    const int w = __builtin_amdgcn_readfirstlane(t >> 6);
    const int b = blockIdx.x;
    const int tile = (b & 7) * (TILES / 8) + (b >> 3);
    const int c16 = lane & 15, q = lane >> 4;
    const int node0 = tile * NT;
    const int nvalid = min(NT, N - node0);
    if (nvalid <= 0) return;
    const u32 lbase = (u32)(size_t)(lds_char*)smem;
    MST_DECL;
    MST(0);

    u32x2 fq;
    aload8(fq, freqs, 0u);
    u32x4 bw;
    aload16(bw, (const char*)bins + (size_t)tile * (G * 64), (u32)t * 16u);
    u32 pdv;
    aload2(pdv, pos + (size_t)node0 * 3, (u32)min(t, 3 * nvalid - 1) * 2u);
    u32x4 W2[3][4];
#pragma unroll
    for (int gg = 0; gg < 3; ++gg)
#pragma unroll
        for (int ks = 0; ks < 4; ++ks) aload16(W2[gg][ks], w2f, (u32)((w * 12 + gg * 4 + ks) * 64 + lane) * 16u);
    u32x2 WR[3];
#pragma unroll
    for (int gg = 0; gg < 3; ++gg) aload8(WR[gg], wrf, (u32)((w * 3 + gg) * 64 + lane) * 8u);
    const char* const rbase = (const char*)sv + (size_t)node0 * 1024;
#define RES_OFF(rb, i) ((u32)min((rb) * 16 + 2 * w + (i), nvalid - 1) * 1024u + (u32)lane * 16u)
#define RES_FIRST()                                                                                         \
    do {                                                                                                    \
        _Pragma("unroll") for (int i = 0; i < 4; ++i)                                                       \
            dma16c(rbase + RES_OFF(i >> 1, i & 1), lbase + L_STG + (u32)((i >> 1) * STAGE_B + (2 * w + (i & 1)) * SPITCH), 1); \
    } while (0)
#define RING_EARLY()                                                                                        \
    do {                                                                                                    \
        RING_LOAD0(rbase, RES_OFF(2, 0), RES_OFF(2, 1));                                                    \
        RING_LOAD1(rbase, RES_OFF(3, 0), RES_OFF(3, 1));                                                    \
    } while (0)
#define RING_FIRST()                                                                                        \
    do {                                                                                                    \
        RING_LOAD0(rbase, RES_OFF(2, 0), RES_OFF(2, 1));                                                    \
        issued += 2; mR0 = issued;                                                                          \
        RING_LOAD1(rbase, RES_OFF(3, 0), RES_OFF(3, 1));                                                    \
        issued += 2; mR1 = issued;                                                                          \
    } while (0)

    u32* const l_tmp = (u32*)(smem + L_BUF + 2 * CHUNK_B);
    u32* const l_fin = (u32*)(smem + L_FIN);
    u32* const l_hist = (u32*)(smem + L_HIST);
    u32* const l_start = (u32*)(smem + L_START);
    float* const l_posd = (float*)(smem + L_POSD);
    u32* const l_misc = (u32*)(smem + L_MISC);
    u16* const l_dl = (u16*)(smem + L_DL);
    float* const l_rh = (float*)(smem + L_RH);

    if (t < 128) l_hist[t] = 0;
    if (t == 128) l_misc[0] = 0;
    lds_barrier();

    VMWAIT(15);
    asm volatile("" : "+v"(bw), "+v"(pdv), "+v"(fq));
    const float fr0 = __uint_as_float(fq[0] << 16), fr1 = __uint_as_float(fq[0] & 0xffff0000u);
    const float fr2 = __uint_as_float(fq[1] << 16), fr3 = __uint_as_float(fq[1] & 0xffff0000u);
    MST(1);
    if (t < 3 * nvalid) l_posd[t] = bf2f(pdv);
    const u32 cnt = (u32)__builtin_amdgcn_update_dpp(0, (int)bw[0], 0x00, 0xf, 0xf, false);
    if (cnt > 15u) l_misc[0] = 1;
    u32 rk[4];
    bool vl[4];
#pragma unroll
    for (int i = 0; i < 4; ++i) {
        const u32 widx = 4 * (t & 3) + i;
        vl[i] = widx >= 1 && widx <= cnt && cnt <= 15u;
        rk[i] = 0;
        if (vl[i]) rk[i] = atomicAdd(&l_hist[bw[i] >> 24], 1u);
    }
    RES_FIRST();
    lds_barrier();
    u32 starts;
    int total;
    {
        const u32x2 hh = *(const u32x2*)(l_hist + 2 * lane);
        const u32 inc = wave_incl_scan(hh[0] + hh[1]);
        const u32 s0 = inc - hh[0] - hh[1], s1 = s0 + hh[0];
        starts = s0 | (s1 << 16);
        total = __builtin_amdgcn_readlane((int)inc, 63);
        if (w == 0) { u32x2 sx; sx[0] = s0; sx[1] = s1; *(u32x2*)(l_start + 2 * lane) = sx; if (lane == 63) l_start[128] = inc; }
    }
    const bool slow = __builtin_amdgcn_readfirstlane((int)l_misc[0]) != 0;
#pragma unroll
    for (int i = 0; i < 4; ++i) {
        const u32 loc = (bw[i] >> 24) & 127u;
        const u32 p = (u32)__builtin_amdgcn_ds_bpermute((int)((loc >> 1) << 2), (int)starts);
        const u32 st = (loc & 1) ? (p >> 16) : (p & 0xffffu);
        if (vl[i]) l_tmp[st + rk[i]] = bw[i];
    }
    if (t >= 480) l_fin[total + t - 480] = 0;
    lds_barrier();
    const u32 posLast = (u32)max(6 * N - 8, 0);
#define POS_OFF(sidx) min((sidx) * 6u, posLast)
    const bool early = !slow && total <= CAP;
    u32 erec = l_tmp[max(min(t, total - 1), 0)];
    if (total == 0) erec = 0;
    u32x2 epos;
    aload8(epos, pos, POS_OFF(erec & 0xffffffu));
    int egp = 0;
    for (int p = t; p < total; p += 512) {
        const u32 rec = l_tmp[p];
        const u32 loc = rec >> 24;
        const int s = (int)l_start[loc], e = (int)l_start[loc + 1];
        int rank = 0;
        for (int j = s; j < e; j += 2) {
            const u32 k0 = l_tmp[j], k1 = l_tmp[j + 1];
            rank += (k0 < rec || (k0 == rec && j < p)) ? 1 : 0;
            rank += (j + 1 < e && (k1 < rec || (k1 == rec && j + 1 < p))) ? 1 : 0;
        }
        l_fin[s + rank] = rec;
        egp = s + rank;
    }
    lds_barrier();
    MST(2);

    f32x4 acc[4];
#pragma unroll
    for (int x = 0; x < 4; ++x) acc[x] = (f32x4){0.f, 0.f, 0.f, 0.f};
    int cur = 0;
    int sto = 0;
    int epoch = 0, eS0 = -1, eS1 = -1;
    int issued = 0;
    int mR0 = 0, mR1 = 0;

    const u32 dshift = lane < 16 ? 8u : 10u;
    const char* const dbase = (lane < 16 ? (const char*)h : (const char*)sv) + lane * 16;
    char* const obase = (char*)out + (size_t)node0 * 1024;
    const u32 kOne = 0x3F803F80u;
    u32x2 ident;
    ident[0] = (4 * q == c16 ? 0x3F80u : 0u) | (4 * q + 1 == c16 ? 0x3F800000u : 0u);
    ident[1] = (4 * q + 2 == c16 ? 0x3F80u : 0u) | (4 * q + 3 == c16 ? 0x3F800000u : 0u);
    u32 tg = (u32)(2 * c16) * 0x10001u;

#define STORE_ONE(d0_, d1_)                                                                                 \
    do {                                                                                                    \
        const u32 la_ = lbase + L_STG + (u32)((sto & 1) * STAGE_B + 2 * w * SPITCH) + (u32)lane * 16u;      \
        asm volatile("" : "+v"(d0_), "+v"(d1_));                                                            \
        if (sto < 5) {                                                                                      \
            if (sto & 1) {                                                                                  \
                wait_vm(issued - mR1);                                                                      \
                RING_WRITE1(la_);                                                                           \
                if (sto < 3) {                                                                              \
                    RING_LOAD1(rbase, RES_OFF(sto + 4, 0), RES_OFF(sto + 4, 1));                            \
                    issued += 2; mR1 = issued;                                                              \
                }                                                                                           \
                eS1 = epoch;                                                                                \
            } else {                                                                                        \
                wait_vm(issued - mR0);                                                                      \
                RING_WRITE0(la_);                                                                           \
                if (sto < 3) {                                                                              \
                    RING_LOAD0(rbase, RES_OFF(sto + 4, 0), RES_OFF(sto + 4, 1));                            \
                    issued += 2; mR0 = issued;                                                              \
                }                                                                                           \
                eS0 = epoch;                                                                                \
            }                                                                                               \
        }                                                                                                   \
        const int row_ = sto * 16 + 2 * w;                                                                  \
        const u32 oo_ = (u32)row_ * 1024u + (u32)lane * 16u;                                                \
        if (row_ < nvalid) { astore16nt(d0_, obase, oo_); ++issued; }                                       \
        if (row_ + 1 < nvalid) { astore16nt(d1_, obase, oo_ + 1024u); ++issued; }                           \
        ++sto;                                                                                              \
    } while (0)
#define STG_ROWS(rb) (smem + L_STG + ((rb) & 1) * STAGE_B + 2 * w * SPITCH + lane * 16)
#define EPOCH_BARRIER()                                                                                     \
    do {                                                                                                    \
        const int lim_ = cur;                                                                               \
        lds_barrier();                                                                                      \
        ++epoch;                                                                                            \
        while (sto < lim_) {                                                                                \
            u32x4 e0_ = *(const u32x4*)STG_ROWS(sto), e1_ = *(const u32x4*)(STG_ROWS(sto) + SPITCH);        \
            STORE_ONE(e0_, e1_);                                                                            \
        }                                                                                                   \
    } while (0)
#define READY() (sto >= cur - 1 && epoch > ((cur & 1) ? eS1 : eS0))
#define RES_READ(rt)                                                                                        \
    _Pragma("unroll") for (int x_ = 0; x_ < 4; ++x_)                                                        \
        rt[x_] = tr_read(smem + L_STG + (cur & 1) * STAGE_B + 32 * w + (4 * q + (c16 >> 2)) * SPITCH + 8 * (c16 & 3) + 256 * x_)
#define FLUSH_CUR(rt, have)                                                                                 \
    do {                                                                                                    \
        if (!(have)) {                                                                                      \
            while (!READY()) EPOCH_BARRIER();                                                               \
            RES_READ(rt);                                                                                   \
        }                                                                                                   \
        char* sb_ = smem + L_STG + (cur & 1) * STAGE_B + 32 * w;                                            \
        _Pragma("unroll") for (int x_ = 0; x_ < 4; ++x_) {                                                  \
            const f32x4 f_ = mfma16k16(rt[x_], ident, acc[x_]);                                             \
            u32x2 o_; o_[0] = pack2(f_[0], f_[1]); o_[1] = pack2(f_[2], f_[3]);                             \
            *(u32x2*)(sb_ + c16 * SPITCH + 8 * q + 256 * x_) = o_;                                          \
        }                                                                                                   \
        ++cur; tg += 32u * 0x10001u;                                                                        \
    } while (0)
#define SEG(rt, have)                                                                                       \
    do {                                                                                                    \
        int endc_ = __builtin_amdgcn_readlane((int)rbtab, cur);                                             \
        if (endc_ > sEb) {                                                                                  \
            const u32x4 S_ = onehot(dA, dB, tg, kOne);                                                      \
            _Pragma("unroll") for (int x_ = 0; x_ < 4; ++x_) acc[x_] = mfma16(A[x_], S_, acc[x_]);          \
        }                                                                                                   \
        bool have_ = (have);                                                                                \
        while (endc_ < sE1) {                                                                               \
            FLUSH_CUR(rt, have_);                                                                           \
            have_ = false;                                                                                  \
            endc_ = __builtin_amdgcn_readlane((int)rbtab, cur);                                             \
            const u32x4 S_ = onehot(dA, dB, tg, kOne);                                                      \
            _Pragma("unroll") for (int x_ = 0; x_ < 4; ++x_)                                                \
                acc[x_] = mfma16(A[x_], S_, (f32x4){0.f, 0.f, 0.f, 0.f});                                   \
        }                                                                                                   \
    } while (0)

    int W0 = 0;
    int slowR = 0, slowE = 0;
    bool first = true;
    for (;;) {
        int nrec;
        u32 rbtab;
        const u32* list;
        if (!slow) {
            if (W0 >= total) break;
            nrec = min(CAP, total - W0);
            list = l_fin + W0;
            const u32 nx = (u32)__builtin_amdgcn_ds_bpermute(((lane & 7) + 1) * 32, (int)starts) & 0xffffu;
            rbtab = (u32)min(max((int)nx - W0, 0), nrec);
            W0 += CAP;
            if (!first) lds_barrier();
        } else {
            int n = 0;
            lds_barrier();
            while (slowR < 7) {
                if (slowE >= E) {
                    ++slowR; slowE = 0;
                    if (n > 0) { --slowR; slowE = E; break; }
                    continue;
                }
                const int e = slowE + t;
                int di = -1, si = 0;
                if (e < E) { di = dst[e]; si = src[e]; }
                asm volatile("" : "+v"(di), "+v"(si));
                const int loc = di - node0;
                const bool m = e < E && loc >= 0 && loc < nvalid && (loc >> 4) == slowR;
                const u64 bal = __ballot(m);
                const int pw = __builtin_amdgcn_mbcnt_hi((u32)(bal >> 32), __builtin_amdgcn_mbcnt_lo((u32)bal, 0));
                if (lane == 0) l_hist[w] = (u32)__popcll(bal);
                lds_barrier();
                int basep = 0, all = 0;
#pragma unroll
                for (int i = 0; i < 8; ++i) { const int c = (int)l_hist[i]; basep += i < w ? c : 0; all += c; }
                all = __builtin_amdgcn_readfirstlane(all);
                const bool fits = n + all <= CAP;
                if (fits && m) l_fin[n + basep + pw] = ((u32)loc << 24) | (u32)si;
                lds_barrier();
                if (!fits) break;
                n += all; slowE += 512;
            }
            if (n == 0) {
                if (slowR >= 7) break;
            }
            if (t < 32) l_fin[n + t] = 0;
            lds_barrier();
            nrec = n;
            list = l_fin;
            rbtab = lane < slowR ? 0u : (u32)n;
            if (slowE >= E) { ++slowR; slowE = 0; }
            if (n == 0) continue;
        }
        nrec = __builtin_amdgcn_readfirstlane(nrec);
        const int nch = (nrec + 31) >> 5;
        int mA, mB, mC;
        u32 bA = lbase + L_BUF, bB = lbase + L_BUF + CHUNK_B, bC = lbase + L_BUF + 2 * CHUNK_B;
        const u32* const listw = list + 4 * w;
#define DMA_ROW(r4, i_, buf, en) dma16c(dbase + ((r4)[i_] << dshift), (buf) + (u32)(4 * w + (i_)) * PITCH, en)
        u32x4 nr4;
#define DMA_PROLOGUE()                                                                                      \
    do {                                                                                                    \
        const u32x4 r0 = *(const u32x4*)listw, r1 = *(const u32x4*)(listw + 32);                            \
        nr4 = *(const u32x4*)(listw + 64);                                                                  \
        _Pragma("unroll") for (int i = 0; i < 4; ++i) DMA_ROW(r0, i, bA, 1);                                \
        issued += 4;                                                                                        \
        mA = issued;                                                                                        \
        const int rem1 = nch - 1, en1 = rem1 > 0 ? 1 : 0;                                                   \
        _Pragma("unroll") for (int i = 0; i < 4; ++i) DMA_ROW(r1, i, bB, rem1);                             \
        issued += 4 * en1;                                                                                  \
        mB = issued;                                                                                        \
        if (first) RING_FIRST();                                                                            \
        mC = issued;                                                                                        \
    } while (0)
#define GEOMETRY(rec, pp, gp)                                                                               \
    do {                                                                                                    \
        const u32 loc = (rec) >> 24, sidx = (rec) & 0xffffffu;                                              \
        const bool shifted = sidx * 6u > posLast;                                                           \
        const u32 pxy = shifted ? __builtin_amdgcn_alignbit((pp)[1], (pp)[0], 16) : (pp)[0];                \
        const u32 pzz = shifted ? (pp)[1] >> 16 : (pp)[1];                                                  \
        const float rx = rbf16(lo16(pxy) - l_posd[3 * loc]);                                                \
        const float ry = rbf16(hi16(pxy) - l_posd[3 * loc + 1]);                                            \
        const float rz = rbf16(lo16(pzz) - l_posd[3 * loc + 2]);                                            \
        const float ss = rbf16(rbf16(rx * rx) + rbf16(ry * ry) + rbf16(rz * rz));                           \
        const float d = rbf16(sqrtf(rbf16(ss + 1e-8f)));                                                    \
        const float inv = __builtin_amdgcn_rcpf(d);                                                         \
        const float rh0 = rbf16(rx * inv), rh1 = rbf16(ry * inv), rh2 = rbf16(rz * inv);                    \
        const float i2pi = 0.15915494309189535f;                                                            \
        float ang = rbf16(rbf16(3.14159265358979323846f * d) / 5.0f);                                       \
        float fc = rbf16(__builtin_amdgcn_cosf(__builtin_amdgcn_fractf(ang * i2pi)));                       \
        fc = rbf16(0.5f * rbf16(fc + 1.0f));                                                                \
        fc = d < 5.0f ? fc : 0.0f * d;                                                                      \
        float cf[4];                                                                                        \
        const float fr[4] = {fr0, fr1, fr2, fr3};                                                           \
        _Pragma("unroll") for (int k = 0; k < 4; ++k) {                                                     \
            const float arg = rbf16(d * fr[k]);                                                             \
            const float sn = rbf16(__builtin_amdgcn_sinf(__builtin_amdgcn_fractf(arg * i2pi)));             \
            cf[k] = rbf16(sn * inv) * fc;                                                                   \
        }                                                                                                   \
        float hi[4], lo[4];                                                                                 \
        _Pragma("unroll") for (int k = 0; k < 4; ++k) { hi[k] = rbf16(cf[k]); lo[k] = cf[k] - hi[k]; }      \
        u32x4 a;                                                                                            \
        a[0] = pack2(hi[0], hi[1]); a[1] = pack2(hi[2], hi[3]); a[2] = pack2(lo[0], lo[1]); a[3] = pack2(lo[2], lo[3]); \
        *(u32x4*)(smem + L_WFA + (gp) * 16) = a;                                                            \
        l_rh[gp] = rh0; l_rh[CAP + (gp)] = rh1; l_rh[2 * CAP + (gp)] = rh2;                                 \
        l_dl[gp] = (u16)(2 * loc);                                                                          \
    } while (0)
        if (early) {
            DMA_PROLOGUE();
            MST(3);
            wait_vm(issued);
            asm volatile("" : "+v"(epos));
            MST(4);
            if (t < nrec) GEOMETRY(erec, epos, egp);
        } else {
            const u32 rec = list[min(t, nrec - 1)];
            u32x2 pp;
            aload8(pp, pos, POS_OFF(rec & 0xffffffu));
            issued += 1;
            const int mPos = issued;
            DMA_PROLOGUE();
            wait_vm(issued - mPos);
            asm volatile("" : "+v"(pp));
            if (t < nrec) GEOMETRY(rec, pp, t);
        }
        if (t >= nrec && t < nch * 32) {
            *(u32x4*)(smem + L_WFA + t * 16) = (u32x4){0u, 0u, 0u, 0u};
            l_rh[t] = 0.f; l_rh[CAP + t] = 0.f; l_rh[2 * CAP + t] = 0.f;
            l_dl[t] = (u16)0x7FFEu;
        }
        if (first) {
#pragma unroll
            for (int gg = 0; gg < 3; ++gg) {
                asm volatile("" : "+v"(WR[gg]));
#pragma unroll
                for (int ks = 0; ks < 4; ++ks) asm volatile("" : "+v"(W2[gg][ks]));
            }
            first = false;
        }
        MST(5);

        u32x4 A[4];
        u32x2 dA, dB;
        int sEb = 0, sE1 = 0;
#pragma unroll
        for (int x = 0; x < 4; ++x) A[x] = (u32x4){0u, 0u, 0u, 0u};
        dA = dB = (u32x2){0u, 0u};
        for (int ch = 0; ch < nch; ++ch) {
            if (ch < 10) MST(8+5*ch);
            wait_vm(issued - mA);
            if (ch < 10) MSTI(9+5*ch);
            lds_barrier();
            ++epoch;
            if (ch < 10) MSTI(10+5*ch);
            const char* B = smem + (bA - lbase);
            const int eb = ch * 32;
            const int stoTop = sto, curTop = cur;
            const bool pref = READY();
            const int rem = nch - ch - 2, en = rem > 0 ? 1 : 0;
            u32x2 wa[2], rt[4];
            u32x4 a0[4], a1[4];
#pragma unroll
            for (int blk = 0; blk < 2; ++blk) wa[blk] = *(const u32x2*)(smem + L_WFA + (eb + 16 * blk + c16) * 16 + (q & 1) * 8);
            RES_READ(rt);
#pragma unroll
            for (int ks = 0; ks < 4; ++ks) a0[ks] = *(const u32x4*)(B + c16 * PITCH + ks * 64 + q * 16);
            u32x4 sd0 = *(const u32x4*)STG_ROWS(sto), sd1 = *(const u32x4*)(STG_ROWS(sto) + SPITCH);
#pragma unroll
            for (int ks = 0; ks < 4; ++ks) a1[ks] = *(const u32x4*)(B + (16 + c16) * PITCH + ks * 64 + q * 16);
            SB();
            if (ch >= 1) SEG(rt, pref);
            if (ch < 10) MSTI(11+5*ch);
            SB();
            u32x2 vv[3];
            f32x4 rh[3];
#define V_READ(blk)                                                                                         \
    _Pragma("unroll") for (int x = 0; x < 3; ++x) {                                                         \
        vv[x] = tr_read(B + (16 * (blk) + 4 * q + (c16 >> 2)) * PITCH + 256 * (1 + x) + 32 * w + 8 * (c16 & 3)); \
        rh[x] = *(const f32x4*)(l_rh + x * CAP + eb + 16 * (blk) + 4 * q);                                  \
    }
#define MSG_HEAD(blk)                                                                                       \
    do {                                                                                                    \
        const f32x4 ms = Z[0] * Wf[0];                                                                      \
        mvv = Z[1] * Wf[1]; mvr = Z[2] * Wf[2];                                                             \
        A[0][2 * (blk)] = pack2(ms[0], ms[1]); A[0][2 * (blk) + 1] = pack2(ms[2], ms[3]);                   \
    } while (0)
#define MSG_V(blk, x)                                                                                       \
    do {                                                                                                    \
        const f32x4 vf = VUNPACK(vv[x]);                                                                    \
        const f32x4 u = mvv * vf + mvr * rh[x];                                                             \
        A[1 + (x)][2 * (blk)] = pack2(u[0], u[1]); A[1 + (x)][2 * (blk) + 1] = pack2(u[2], u[3]);           \
    } while (0)
#define VUNPACK(v) ((f32x4){lo16((v)[0]), hi16((v)[0]), lo16((v)[1]), hi16((v)[1])})
            f32x4 Z[3], Wf[3], Y[3], Wg[3], mvv, mvr;
            V_READ(0);
#pragma unroll
            for (int gg = 0; gg < 3; ++gg) Z[gg] = mfma16(a0[0], W2[gg][0], (f32x4){0.f, 0.f, 0.f, 0.f});
#pragma unroll
            for (int gg = 0; gg < 3; ++gg) Z[gg] = mfma16(a0[1], W2[gg][1], Z[gg]);
            SB();
            DMA_ROW(nr4, 0, bC, rem);
            issued += en;
            SB();
#pragma unroll
            for (int gg = 0; gg < 3; ++gg) Wf[gg] = mfma16k16(wa[0], WR[gg], (f32x4){0.f, 0.f, 0.f, 0.f});
#pragma unroll
            for (int gg = 0; gg < 3; ++gg) Z[gg] = mfma16(a0[2], W2[gg][2], Z[gg]);
#pragma unroll
            for (int gg = 0; gg < 3; ++gg) Z[gg] = mfma16(a0[3], W2[gg][3], Z[gg]);
            SB();
            DMA_ROW(nr4, 1, bC, rem);
            issued += en;
            SB();
            if (sto == stoTop && stoTop < curTop) STORE_ONE(sd0, sd1);
            if (ch < 10) MSTI(12+5*ch);
            SB();
#pragma unroll
            for (int gg = 0; gg < 3; ++gg) Y[gg] = mfma16(a1[0], W2[gg][0], (f32x4){0.f, 0.f, 0.f, 0.f});
            MSG_HEAD(0);
            SB();
#pragma unroll
            for (int gg = 0; gg < 3; ++gg) Y[gg] = mfma16(a1[1], W2[gg][1], Y[gg]);
            MSG_V(0, 0);
            SB();
            DMA_ROW(nr4, 2, bC, rem);
            issued += en;
            SB();
#pragma unroll
            for (int gg = 0; gg < 3; ++gg) Y[gg] = mfma16(a1[2], W2[gg][2], Y[gg]);
            MSG_V(0, 1);
            SB();
#pragma unroll
            for (int gg = 0; gg < 3; ++gg) Y[gg] = mfma16(a1[3], W2[gg][3], Y[gg]);
            MSG_V(0, 2);
            SB();
            V_READ(1);
            dA = *(const u32x2*)(l_dl + eb + 4 * q); dB = *(const u32x2*)(l_dl + eb + 16 + 4 * q);
#pragma unroll
            for (int gg = 0; gg < 3; ++gg) Wg[gg] = mfma16k16(wa[1], WR[gg], (f32x4){0.f, 0.f, 0.f, 0.f});
            SB();
            DMA_ROW(nr4, 3, bC, rem);
            issued += en; mC = issued;
            nr4 = *(const u32x4*)(listw + (ch + 3) * 32);
            SB();
#pragma unroll
            for (int gg = 0; gg < 3; ++gg) { Z[gg] = Y[gg]; Wf[gg] = Wg[gg]; }
            MSG_HEAD(1);
            MSG_V(1, 0); MSG_V(1, 1); MSG_V(1, 2);
            sEb = eb; sE1 = min(eb + 32, nrec);
            mA = mB; mB = mC;
            const u32 tb = bA; bA = bB; bB = bC; bC = tb;
        }
        {
            u32x2 rt[4];
            SEG(rt, false);
        }
    }
    MST(6);
    if (first) {
        RING_FIRST();
        VMWAIT(0);
        lds_barrier();
        ++epoch;
    }
    while (cur < 7) {
        u32x2 rt[4];
        FLUSH_CUR(rt, false);
#pragma unroll
        for (int x = 0; x < 4; ++x) acc[x] = (f32x4){0.f, 0.f, 0.f, 0.f};
    }
    EPOCH_BARRIER();
    MST(7);
    MST_OUT();
}

extern "C" void kernel_launch(void* const* d_in, const int* in_sizes, int n_in, void* d_out, int out_size, void* d_ws,
                              size_t ws_size, hipStream_t stream) {
    const u16* sv = (const u16*)d_in[0];
    const u16* pos = (const u16*)d_in[1];
    const int* src = (const int*)d_in[2];
    const int* dst = (const int*)d_in[3];
    const u16* freqs = (const u16*)d_in[4];
    const u16* w1 = (const u16*)d_in[5];
    const u16* w2 = (const u16*)d_in[6];
    const u16* wrbf = (const u16*)d_in[7];
    u16* out = (u16*)d_out;
    const int N = in_sizes[0] / 512;
    const int E = in_sizes[2];
    const int NT = (N + TILES - 1) / TILES;
    const int EPB = ((E + G - 1) / G + 3) / 4 * 4;
    const u32 ntmagic = (u32)((0x100000000ull + NT - 1) / NT);
    const int nNodeBlocks = (N + NODES_PB - 1) / NODES_PB;

    char* ws = (char*)d_ws;
    size_t off = 0;
    u16* h = (u16*)(ws + off); off += ((size_t)N * 256 + 255) / 256 * 256;
    u32x4* w2f = (u32x4*)(ws + off); off += 6144 * 16;
    u32x2* wrf = (u32x2*)(ws + off); off += 24 * 64 * 8;
    u32* bins = (u32*)(ws + off);

    const int posBytes = ((N * 6 + 15) / 16) * 16;
    int preLds = posBytes + TILES * 4 + 64 + (posBytes >= 16384 ? 0 : 16384);
    if (preLds < 32768) preLds = 32768;
    (void)hipFuncSetAttribute((const void*)pre_kernel, hipFuncAttributeMaxDynamicSharedMemorySize, preLds);
    (void)hipFuncSetAttribute((const void*)main_kernel, hipFuncAttributeMaxDynamicSharedMemorySize, MAIN_LDS);
    pre_kernel<<<G + nNodeBlocks + W2_BLOCKS, PRE_T, preLds, stream>>>(sv, pos, src, dst, w1, w2, wrbf, h, w2f, wrf, bins, N, E,
                                                                       EPB, NT, ntmagic, nNodeBlocks);
    main_kernel<<<TILES, 512, MAIN_LDS, stream>>>(sv, pos, src, dst, freqs, h, w2f, wrf, bins, out, N, E, NT);
}
```
